```python
import torch

def _softmax(x, axis=-1):
    return torch.softmax(x, dim=axis)
def _gelu(x):
    return torch.nn.functional.gelu(x, approximate="tanh")
def _silu(x):
    return torch.nn.functional.silu(x)
def _sigmoid(x):
    return torch.sigmoid(x)
def _layernorm(x, axis=-1, eps=1e-5):
    m = x.mean(dim=axis, keepdim=True)
    v = x.var(dim=axis, keepdim=True, unbiased=False)
    return (x - m) / torch.sqrt(v + eps)
def _scatter_add(tgt, idx, src, axis=0):
    return tgt.clone().index_add_(axis, idx, src)
def _segment_sum(src, seg, n):
    out = torch.zeros((n,) + src.shape[1:], dtype=src.dtype)
    return out.index_add_(0, seg, src)
def _pad(x, pw):
    flat = tuple(v for pair in reversed(pw) for v in pair)
    return torch.nn.functional.pad(x, flat)

kernel_name = 'parallel_sum__softmax_full_mha__dense_activation_squared_relu__prompt'

def setup_inputs():

    _w = (lambda a: (lambda t: t.to(torch.bfloat16) if t.is_floating_point() else t)(torch.as_tensor(a)))
    def _A_setup():
        B, S, D = 1, 2048, 640
        return {
            "x": _w((lambda *s: torch.randn(*s, dtype=torch.bfloat16) * 0.05)(B, S, D)),
            "wqkv": _w((lambda *s: torch.randn(*s, dtype=torch.bfloat16) * 0.2)(D, 3 * D)),
            "wo": _w((lambda *s: torch.randn(*s, dtype=torch.bfloat16) * 0.1)(D, D)),
        }
    _A_in = _A_setup()

    def _B_setup():
        B, S, D, F = 1, 2048, 640, 1536
        return {
            "x": _w((lambda *s: torch.randn(*s, dtype=torch.bfloat16) * 0.05)(B, S, D)),
            "w1": _w((lambda *s: torch.randn(*s, dtype=torch.bfloat16) * 0.1)(D, F)),
            "b1": _w((lambda *s: torch.randn(*s, dtype=torch.bfloat16) * 0.1)(F)),
            "w2": _w((lambda *s: torch.randn(*s, dtype=torch.bfloat16) * 0.1)(F, D)),
            "b2": _w((lambda *s: torch.randn(*s, dtype=torch.bfloat16) * 0.1)(D)),
        }
    _B_in = _B_setup()

    return {
        "A_x": _A_in["x"],
        "A_wqkv": _A_in["wqkv"],
        "A_wo": _A_in["wo"],
        "B_x": _B_in["x"],
        "B_w1": _B_in["w1"],
        "B_b1": _B_in["b1"],
        "B_w2": _B_in["w2"],
        "B_b2": _B_in["b2"],
    }

def reference(A_x, A_wqkv, A_wo, B_x, B_w1, B_b1, B_w2, B_b2):
    def _A_ref(x, wqkv, wo):
        B, S, D = x.shape
        H = 32; Dh = D // H
        qkv = (lambda x, *d: x.permute(*d))((x @ wqkv).reshape(B, S, 3, H, Dh), 2, 0, 3, 1, 4)
        q, k, v = qkv[0], qkv[1], qkv[2]
        scale = Dh ** -0.5
        scores = torch.einsum("bhqd,bhkd->bhqk", q, k) * scale
        p = _softmax(scores, axis=-1)
        o = torch.einsum("bhqk,bhkd->bhqd", p, v)
        return (lambda x, *d: x.permute(*d))(o, 0, 2, 1, 3).reshape(B, S, D) @ wo

    def _B_ref(x, w1, b1, w2, b2):
        h = torch.einsum("bsd,df->bsf", x, w1) + b1
        zero = torch.zeros_like(h)
        r = torch.where(h > zero, h, zero)
        h = r * r
        out = torch.einsum("bsf,fd->bsd", h, w2) + b2
        return out

    x = A_x
    xn = _layernorm(x, axis=-1)
    return x + _A_ref(xn, A_wqkv, A_wo) + _B_ref(xn, B_w1, B_b1, B_w2, B_b2)
```

```cpp
#include <hip/hip_runtime.h>
#include <hip/hip_bf16.h>
typedef __hip_bfloat16 bf16;
constexpr int S = 2048, D = 640, F = 1536, H = 32, DH = 20;
__device__ __forceinline__ float b2f(bf16 v) { return __bfloat162float(v); }
__global__ void k_ln(const bf16* x, float* xn) {
  int r = blockIdx.x; __shared__ float red[2][4]; float v[3], s = 0, q = 0;
  for (int i = 0; i < 3; ++i) { int c = threadIdx.x + i * 256; v[i] = c < D ? b2f(x[r * D + c]) : 0.f; s += v[i]; }
  for (int o = 32; o; o >>= 1) s += __shfl_xor(s, o);
  if ((threadIdx.x & 63) == 0) red[0][threadIdx.x >> 6] = s; __syncthreads();
  float m = (red[0][0] + red[0][1] + red[0][2] + red[0][3]) / D;
  for (int i = 0; i < 3; ++i) { int c = threadIdx.x + i * 256; if (c < D) q += (v[i] - m) * (v[i] - m); }
  for (int o = 32; o; o >>= 1) q += __shfl_xor(q, o);
  if ((threadIdx.x & 63) == 0) red[1][threadIdx.x >> 6] = q; __syncthreads();
  float rs = rsqrtf((red[1][0] + red[1][1] + red[1][2] + red[1][3]) / D + 1e-5f);
  for (int i = 0; i < 3; ++i) { int c = threadIdx.x + i * 256; if (c < D) xn[r * D + c] = (v[i] - m) * rs; }
}
__global__ void k_gemm(const float* A, const bf16* W, const bf16* bias, float* C, int K, int N, int mode) {
  __shared__ float As[16][17], Ws[16][17]; int tx = threadIdx.x & 15, ty = threadIdx.x >> 4; int row = blockIdx.y * 16 + ty, col = blockIdx.x * 16 + tx; float acc = 0;
  for (int k0 = 0; k0 < K; k0 += 16) { As[ty][tx] = A[(size_t)row * K + k0 + tx]; Ws[ty][tx] = b2f(W[(size_t)(k0 + ty) * N + col]); __syncthreads();
    for (int k = 0; k < 16; ++k) acc += As[ty][k] * Ws[k][tx]; __syncthreads(); }
  if (mode == 1) { acc += b2f(bias[col]); acc = acc > 0 ? acc * acc : 0; }
  if (mode != 2) acc = b2f(__float2bfloat16(acc));
  C[(size_t)row * N + col] = acc;
}
__global__ void k_attn(const float* qkv, float* o) {
  int h = blockIdx.y, q = blockIdx.x * 64 + threadIdx.x; float qv[DH], acc[DH], m = -1e30f, l = 0; __shared__ float ks[64][DH], vs[64][DH];
  for (int d = 0; d < DH; ++d) { qv[d] = qkv[(size_t)q * 3 * D + h * DH + d] * 0.22360679775f; acc[d] = 0; }
  for (int k0 = 0; k0 < S; k0 += 64) { __syncthreads(); for (int d = 0; d < DH; ++d) { ks[threadIdx.x][d] = qkv[(size_t)(k0 + threadIdx.x) * 3 * D + D + h * DH + d]; vs[threadIdx.x][d] = qkv[(size_t)(k0 + threadIdx.x) * 3 * D + 2 * D + h * DH + d]; } __syncthreads();
    for (int k = 0; k < 64; ++k) { float s = 0;
#pragma unroll
      for (int d = 0; d < DH; ++d) s += qv[d] * ks[k][d];
      float mn = fmaxf(m, s), a = __expf(m - mn), p = __expf(s - mn); l = l * a + p; m = mn;
#pragma unroll
      for (int d = 0; d < DH; ++d) acc[d] = acc[d] * a + p * vs[k][d]; } }
  for (int d = 0; d < DH; ++d) o[(size_t)q * D + h * DH + d] = b2f(__float2bfloat16(acc[d] / l));
}
__global__ void k_fin(const bf16* x, const float* a, const float* b, const bf16* b2, bf16* out) { int i = blockIdx.x * 256 + threadIdx.x; out[i] = __float2bfloat16(b2f(x[i]) + a[i] + b[i] + b2f(b2[i % D])); }
extern "C" void kernel_launch(void* const* d_in, const int* in_sizes, int n_in, void* d_out, int out_size, void* d_ws, size_t ws_size, hipStream_t stream) {
  const bf16 *x = (const bf16*)d_in[0], *wqkv = (const bf16*)d_in[1], *wo = (const bf16*)d_in[2], *w1 = (const bf16*)d_in[4], *b1 = (const bf16*)d_in[5], *w2 = (const bf16*)d_in[6], *b2 = (const bf16*)d_in[7];
  float* ws = (float*)d_ws; float *xn = ws, *qkv = xn + S * D, *hh = qkv + S * 3 * D, *o = hh + S * F, *a = o + S * D, *b = a + S * D;
  k_ln<<<S, 256, 0, stream>>>(x, xn);
  k_gemm<<<dim3(3 * D / 16, S / 16), 256, 0, stream>>>(xn, wqkv, nullptr, qkv, D, 3 * D, 0);
  k_gemm<<<dim3(F / 16, S / 16), 256, 0, stream>>>(xn, w1, b1, hh, D, F, 1);
  k_attn<<<dim3(S / 64, H), 64, 0, stream>>>(qkv, o);
  k_gemm<<<dim3(D / 16, S / 16), 256, 0, stream>>>(o, wo, nullptr, a, D, D, 2);
  k_gemm<<<dim3(D / 16, S / 16), 256, 0, stream>>>(hh, w2, nullptr, b, F, D, 2);
  k_fin<<<S * D / 256, 256, 0, stream>>>(x, a, b, b2, (bf16*)d_out);
}
```

```cpp
#include <hip/hip_runtime.h>
#include <stdint.h>

typedef float    f32x2  __attribute__((ext_vector_type(2)));
typedef float    f32x4  __attribute__((ext_vector_type(4)));
typedef float    f32x16 __attribute__((ext_vector_type(16)));
typedef unsigned u32x2  __attribute__((ext_vector_type(2)));
typedef unsigned u32x4  __attribute__((ext_vector_type(4)));
typedef unsigned u32x8  __attribute__((ext_vector_type(8)));
typedef short    s16x4  __attribute__((ext_vector_type(4)));
typedef __bf16   bf16x2 __attribute__((ext_vector_type(2)));
typedef __bf16   bf16x8 __attribute__((ext_vector_type(8)));
typedef __bf16   bf16x16 __attribute__((ext_vector_type(16)));
typedef unsigned long long u64;
#define LAS __attribute__((address_space(3)))
#define GAS __attribute__((address_space(1)))
#define DI __device__ __forceinline__

constexpr unsigned WS_EPOCH = 0, WS_FLAGA = 4096, WS_FLAGH = WS_FLAGA + 65536, WS_PROF = WS_FLAGH + 65536;
constexpr unsigned WS_ST = WS_PROF + 98304 + 4096;
constexpr unsigned WS_HINT = WS_ST + 32768;
constexpr unsigned WS_KV = 1u << 19;
constexpr unsigned WS_Q  = WS_KV + 32u * 163840u;
constexpr unsigned WS_H  = WS_Q + 32u * 81920u;
constexpr unsigned WS_O  = WS_H + 128u * 48u * 1024u;
constexpr unsigned WS_WP = WS_O + 128u * 20480u;
constexpr unsigned WS_END = WS_WP + 8u * 348160u;
constexpr unsigned L_W = 0, L_Q = 3 * 16384, L_STAT = L_Q + 10240, L_SYNC = L_STAT + 2048, L_X = L_Q + 16384, L_KV0 = 133120, L_RED = 102400, LDS_BYTES = 163840;
static_assert(WS_O == 14680064 && WS_HINT == 270336, "the asm blocks carry these offsets");
constexpr unsigned SPIN_CAP = 1u << 18;
constexpr unsigned ONE2 = 0x3f803f80u;

DI unsigned rfl(unsigned v) { return __builtin_amdgcn_readfirstlane(v); }
DI const char* rflp(const char* p) { const u64 v = (u64)p; return (const char*)(((u64)rfl((unsigned)(v >> 32)) << 32) | rfl((unsigned)v)); }
DI unsigned cvtpk(float lo, float hi) { f32x2 v = {lo, hi}; return __builtin_bit_cast(unsigned, __builtin_convertvector(v, bf16x2)); }
DI float dot2(unsigned a, unsigned b, float c) { return __builtin_amdgcn_fdot2_f32_bf16(__builtin_bit_cast(bf16x2, a), __builtin_bit_cast(bf16x2, b), c, false); }
DI float sum16(float v) {
  v += __uint_as_float(__builtin_amdgcn_update_dpp(0u, __float_as_uint(v), 0x128, 0xf, 0xf, false)); v += __uint_as_float(__builtin_amdgcn_update_dpp(0u, __float_as_uint(v), 0x124, 0xf, 0xf, false));
  v += __uint_as_float(__builtin_amdgcn_update_dpp(0u, __float_as_uint(v), 0x122, 0xf, 0xf, false)); v += __uint_as_float(__builtin_amdgcn_update_dpp(0u, __float_as_uint(v), 0x121, 0xf, 0xf, false)); return v; }
DI float max_h(float v) { auto b = __builtin_amdgcn_permlane32_swap(__float_as_uint(v), __float_as_uint(v), false, false); return fmaxf(__uint_as_float(b[0]), __uint_as_float(b[1])); }
DI u32x2 ldstr(unsigned a) { return __builtin_bit_cast(u32x2, __builtin_amdgcn_ds_read_tr16_b64_v4i16((LAS s16x4*)a)); }
DI f32x16 mfma32(u32x4 a, u32x4 b, f32x16 c) { return __builtin_amdgcn_mfma_f32_32x32x16_bf16(__builtin_bit_cast(bf16x8, a), __builtin_bit_cast(bf16x8, b), c, 0, 0, 0); }
DI f32x16 mfma32k8(u32x2 a, u32x2 b, f32x16 c) { return __builtin_amdgcn_mfma_f32_32x32x8bf16_1k(__builtin_bit_cast(s16x4, a), __builtin_bit_cast(s16x4, b), c, 0, 0, 0); }
DI void bar() { asm volatile("s_waitcnt lgkmcnt(0)\n\ts_barrier" ::: "memory"); }
DI void flag_store(char* p, u64 v) { __hip_atomic_store((GAS u64*)p, v, __ATOMIC_RELAXED, __HIP_MEMORY_SCOPE_AGENT); }
DI u64 flag_load(const char* p) { return __hip_atomic_load((GAS u64*)p, __ATOMIC_RELAXED, __HIP_MEMORY_SCOPE_AGENT); }

typedef float f32x8 __attribute__((ext_vector_type(8)));
typedef unsigned u32x16 __attribute__((ext_vector_type(16)));
DI void rawbar() { asm volatile("s_barrier" ::: "memory"); }
DI u64 read_tag(const char* ws) { const u64 e = flag_load(ws + WS_EPOCH); return (((u64)rfl((unsigned)(e >> 32)) << 32) | rfl((unsigned)e)) + 1; }
DI unsigned fidx(unsigned r, unsigned cc) { return cc * 512u + r * 32u; }

typedef u64 u64x8 __attribute__((ext_vector_type(8)));
__global__ void __launch_bounds__(768, 3) fused_block(const char*, const char*, const char*, const char*, const char*, const char*, const char*, char*, char*) {
  u64x8 ka; u64 kb, pc0; unsigned c, e24; const unsigned wid = rfl(threadIdx.x) >> 6;
  asm volatile(
    "s_load_dwordx16 s[52:67], s[0:1], 0\n"
    "s_load_dwordx2 s[68:69], s[0:1], 64\n"
    "s_getpc_b64 s[70:71]\n"
    "s_lshr_b32 s27, s2, 3\n"
    "s_cmp_lg_u32 s19, 8\n"
    "s_cbranch_scc1 2f\n"
    "v_mbcnt_lo_u32_b32 v22, -1, 0\n"
    "v_lshlrev_b32 v22, 5, v22\n"
    "v_add_lshl_u32 v22, v22, s27, 7\n"
    "v_min_u32 v22, 28160, v22\n"
    "s_mov_b64 exec, 0xff\n"
    "global_load_dword v23, v22, s[70:71]\n"
    "s_mov_b64 exec, -1\n"
    "2:\n"
    "s_add_u32 s36, s27, 4\n"
    "s_lshr_b32 s36, s36, 5\n"
    "s_and_b32 s36, s36, s27\n"
    "s_bfe_u32 s35, s2, 0x10002\n"
    "s_xor_b32 s35, s35, s36\n"
    "s_lshr_b32 s36, s2, 5\n"
    "s_lshl3_add_u32 s35, s35, s36\n"
    "s_sub_u32 s39, s19, 4\n"
    "s_cmp_eq_u32 s35, 15\n"
    "s_cselect_b32 s36, 4, s39\n"
    "s_cmp_ge_u32 s36, 4\n"
    "s_cbranch_scc1 1f\n"
    "v_mbcnt_lo_u32_b32 v22, -1, 0\n"
    "v_mbcnt_hi_u32_b32 v22, -1, v22\n"
    "v_lshrrev_b32 v23, 4, v22\n"
    "v_lshrrev_b32 v24, 3, v22\n"
    "v_lshl_add_u32 v24, s39, 5, v24\n"
    "v_mul_u32_u24 v24, 1280, v24\n"
    "v_xor_b32 v25, v22, v23\n"
    "v_and_b32 v25, 7, v25\n"
    "v_lshl_add_u32 v14, v25, 4, v24\n"
    "v_xor_b32 v15, 64, v14\n"
    "v_add_u32 v16, 18432, v14\n"
    "v_add_u32 v17, 27648, v15\n"
    "v_add_u32 v15, 9216, v15\n"
    "v_lshlrev_b32 v25, 1, v23\n"
    "v_xor_b32 v25, v25, v22\n"
    "v_and_b32 v25, 15, v25\n"
    "v_lshl_add_u32 v23, s39, 4, v23\n"
    "v_mul_u32_u24 v24, 3840, v23\n"
    "v_lshl_add_u32 v18, v25, 4, v24\n"
    "v_xor_b32 v20, 128, v18\n"
    "v_add_u32 v19, 14336, v18\n"
    "v_add_u32 v21, 43008, v20\n"
    "v_add_u32 v20, 28672, v20\n"
    "s_and_b32 s36, s2, 3\n"
    "s_and_b32 s27, s27, 3\n"
    "s_lshl2_add_u32 s36, s36, s27\n"
    "s_mul_i32 s46, s36, 163840\n"
    "s_lshl_b32 s48, s35, 8\n"
    "s_mul_i32 s24, s2, 7\n"
    "s_mul_i32 s36, s24, 0xcccd\n"
    "s_lshr_b32 s36, s36, 19\n"
    "s_mul_i32 s36, s36, 10\n"
    "s_sub_u32 s24, s24, s36\n"
    "s_lshl_b32 s26, s39, 12\n"
    "s_add_u32 s25, s26, 65536\n"
    "s_waitcnt lgkmcnt(0)\n"
    "s_add_u32 s46, s52, s46\n"
    "s_addc_u32 s47, s53, 0\n"
    "s_add_u32 s48, s54, s48\n"
    "s_addc_u32 s49, s55, 0\n"
    "s_mov_b32 m0, s25\n"
    "s_lshl_b32 s36, s24, 7\n"
    "s_add_u32 s44, s46, s36\n"
    "s_addc_u32 s45, s47, 0\n"
    "global_load_lds_dwordx4 v14, s[44:45] offset:0\n"
    "global_load_lds_dwordx4 v15, s[44:45] offset:1024\n"
    "global_load_lds_dwordx4 v16, s[44:45] offset:2048\n"
    "global_load_lds_dwordx4 v17, s[44:45] offset:3072\n"
    "s_mov_b32 m0, s26\n"
    "s_mul_i32 s36, s24, 245760\n"
    "s_add_u32 s44, s48, s36\n"
    "s_addc_u32 s45, s49, 0\n"
    "global_load_lds_dwordx4 v18, s[44:45] offset:0\n"
    "global_load_lds_dwordx4 v19, s[44:45] offset:1024\n"
    "global_load_lds_dwordx4 v20, s[44:45] offset:2048\n"
    "global_load_lds_dwordx4 v21, s[44:45] offset:3072\n"
    "s_add_u32 s25, s25, 16384\n"
    "s_cmp_ge_u32 s25, 163840\n"
    "s_cselect_b32 s36, 98304, 0\n"
    "s_sub_u32 s25, s25, s36\n"
    "s_add_u32 s26, s26, 16384\n"
    "s_cmp_ge_u32 s26, 49152\n"
    "s_cselect_b32 s36, 49152, 0\n"
    "s_sub_u32 s26, s26, s36\n"
    "s_add_u32 s24, s24, 1\n"
    "s_cmp_ge_u32 s24, 10\n"
    "s_cselect_b32 s36, 10, 0\n"
    "s_sub_u32 s24, s24, s36\n"
    "s_mov_b32 m0, s25\n"
    "s_lshl_b32 s36, s24, 7\n"
    "s_add_u32 s44, s46, s36\n"
    "s_addc_u32 s45, s47, 0\n"
    "global_load_lds_dwordx4 v14, s[44:45] offset:0\n"
    "global_load_lds_dwordx4 v15, s[44:45] offset:1024\n"
    "global_load_lds_dwordx4 v16, s[44:45] offset:2048\n"
    "global_load_lds_dwordx4 v17, s[44:45] offset:3072\n"
    "s_mov_b32 m0, s26\n"
    "s_mul_i32 s36, s24, 245760\n"
    "s_add_u32 s44, s48, s36\n"
    "s_addc_u32 s45, s49, 0\n"
    "global_load_lds_dwordx4 v18, s[44:45] offset:0\n"
    "global_load_lds_dwordx4 v19, s[44:45] offset:1024\n"
    "global_load_lds_dwordx4 v20, s[44:45] offset:2048\n"
    "global_load_lds_dwordx4 v21, s[44:45] offset:3072\n"
    "s_add_u32 s25, s25, 16384\n"
    "s_cmp_ge_u32 s25, 163840\n"
    "s_cselect_b32 s36, 98304, 0\n"
    "s_sub_u32 s25, s25, s36\n"
    "s_add_u32 s26, s26, 16384\n"
    "s_cmp_ge_u32 s26, 49152\n"
    "s_cselect_b32 s36, 49152, 0\n"
    "s_sub_u32 s26, s26, s36\n"
    "s_add_u32 s24, s24, 1\n"
    "s_cmp_ge_u32 s24, 10\n"
    "s_cselect_b32 s36, 10, 0\n"
    "s_sub_u32 s24, s24, s36\n"
    "s_branch 3f\n"
    "1:\n"
    "s_waitcnt vmcnt(0) lgkmcnt(0)\n"
    "3:\n"
    : "={s[52:67]}"(ka), "={s[68:69]}"(kb), "={s[70:71]}"(pc0), "={s35}"(c), "={s24}"(e24)
    : "{s[0:1]}"(__builtin_amdgcn_kernarg_segment_ptr()), "{s2}"(blockIdx.x), "{s19}"(wid)
    : "v14", "v15", "v16", "v17", "v18", "v19", "v20", "v21", "v22", "v23", "v24", "v25", "s25", "s26", "s27", "s36", "s39", "s44", "s45", "s46", "s47", "s48", "s49", "scc", "memory");
  const char *x = (const char*)ka[0], *wqkv = (const char*)ka[1], *wo = (const char*)ka[2], *w1 = (const char*)ka[3], *b1 = (const char*)ka[4], *w2 = (const char*)ka[5], *b2 = (const char*)ka[6];
  char *out = (char*)ka[7], *ws = (char*)kb;
#define LANE_VARS unsigned lane; asm volatile("v_mbcnt_lo_u32_b32 %0, -1, 0\n\tv_mbcnt_hi_u32_b32 %0, -1, %0" : "=v"(lane)); __builtin_assume(lane < 64); const unsigned tid = wid * 64 + lane; (void)tid; const unsigned l15 = lane & 15, g = lane >> 4, q4 = (lane >> 2) & 3, p4 = lane & 3; (void)l15; (void)g; (void)q4; (void)p4;
  const unsigned b = blockIdx.x, xcd = b & 7, bi = b >> 3;
  const unsigned rb = (xcd & 3) * 4 + (bi & 3);
  const bool has2 = (c & 7) < 6; const unsigned p2 = c - 2 * (c >> 3);
  const unsigned pj = rb * 4 + (c & 1) + 2 * (c >> 3), pcf = pj & 7, part = pj >> 3;
  const unsigned head = xcd * 4 + (bi & 3), qb = bi >> 2;
  const unsigned rt = (xcd & 3) * 8 + (bi & 7), cf = (xcd >> 2) * 4 + (bi >> 3);
  const __amdgpu_buffer_rsrc_t wsr = __builtin_amdgcn_make_buffer_rsrc(ws, 0, WS_END, 0x00020000);
  const unsigned role = wid >> 2, w4 = wid & 3, wm = w4 >> 1, wn = w4 & 1;
#define GEMM_ADDR(XS, WSL) \
  const unsigned axb = L_X + (XS) * 16384 + (64 * wm + l15) * 128 + ((g ^ (l15 >> 1)) << 4), ax0 = axb + wn * 4096, ax1 = axb + (1 - wn) * 4096; u32x4 aw; const u32x4 ax = {ax0, ax1, ax0 ^ 64, ax1 ^ 64}; \
  _Pragma("unroll") for (int n = 0; n < 4; ++n) aw[n] = L_W + (WSL) * 16384 + (8 * g + q4) * 256 + (((4 * wn + (n ^ (2 * wm))) ^ (q4 | ((g & 1) << 2))) << 5) + p4 * 8;

  if (__builtin_expect(c == 15 && role != 2, 0)) {
    LANE_VARS
    u32x4 xv[4][5];
#pragma unroll
    for (int i = 0; i < 4; ++i)
#pragma unroll
      for (int j = 0; j < 5; ++j) xv[i][j] = *(const GAS u32x4*)(x + (rb * 128 + wid * 16 + i * 4 + g) * 1280 + j * 256 + l15 * 16);
    const unsigned tl = (unsigned)read_tag(ws);
    float mu = 0.f, rs = 0.f;
#pragma unroll
    for (int i = 0; i < 4; ++i) { float s1 = 0.f, s2 = 0.f;
#pragma unroll
      for (int j = 0; j < 5; ++j)
#pragma unroll
        for (int k = 0; k < 4; ++k) { s1 = dot2(xv[i][j][k], ONE2, s1); s2 = dot2(xv[i][j][k], xv[i][j][k], s2); }
      s1 = sum16(s1) * (1.f / 640.f); s2 = sum16(s2) * (1.f / 640.f);
      if (l15 == i) { mu = -s1; rs = __builtin_amdgcn_rsqf(fmaxf(s2 - s1 * s1, 0.f) + 1e-5f); } }
    if (l15 < 4) { char* p = ws + WS_ST + (rb * 128 + wid * 16 + l15 * 4 + g) * 16;
      flag_store(p, ((u64)tl << 32) | __float_as_uint(mu)); flag_store(p + 8, ((u64)tl << 32) | __float_as_uint(rs)); }
  }
  if (__builtin_expect(c == 15 && role == 0, 0)) rawbar();
  f32x8 xb; unsigned xo, a0; float r2;
  if (role != 2) {
    u32x4 va, vb, vc, vd; asm volatile("" : "=v"(va), "=v"(vb), "=v"(vc), "=v"(vd)); unsigned ve, vf = 0, s24 = 0, s30 = 0, s31; const char *pa, *pb, *pc;
    if (role == 1) {
      ve = L_SYNC + w4 * 4; s24 = e24; s31 = part < 6 ? 40 : 50;
      pa = x + rb * 163840u; pb = wqkv + c * 256; pc = has2 ? w1 + p2 * 256 : (part < 6 ? w2 + part * 327680 : wo + (part - 6) * 409600) + pcf * 160;
    } else {
      LANE_VARS
      GEMM_ADDR(0, 0)
      vc = ax; vd = aw; vf = L_STAT + 1024 + (64 * wn + 32 * wm + l15) * 4; ve = L_Q + w4 * 1024 + lane * 16;
      const unsigned er = L_STAT + (64 * wm + (c >= 10 ? 4 * g : l15)) * 4, ec = L_STAT + 1024 + (64 * wn + (c >= 10 ? l15 : 4 * g)) * 4;
      vb = u32x4{er + 128 * wn, er + 128 * (1 - wn), ec + 128 * wm, ec + 128 * (1 - wm)};
      const unsigned clo = (head * 20) >> 7, chi = (head * 20 + 19) >> 7; unsigned cq = (lane & 4) ? chi : clo;
      asm volatile("" : "+v"(cq));
      va = u32x4{lane * 16, WS_FLAGA + fidx(0, 5 + ((lane & 32) ? chi : clo)) + (lane & 31) * 16, WS_FLAGA + fidx(lane < 16 ? 2 * qb + ((lane >> 1) & 1) : rb, lane < 16 ? cq : c) + (lane & 1) * 16, 0};
      s30 = c < 5 ? 0x3ea52b5cu : 0x3f800000u;
      s31 = (1 - 2 * wn) * (c < 5 ? 1280 : 2560);
      pa = ws + WS_FLAGA + fidx(rb, c) + w4 * 8; pb = ws + WS_Q + head * 81920u + qb * 10240u; pc = ws + WS_KV + head * 163840u;
    }
    pa = rflp(pa); pb = rflp(pb); pc = rflp(pc); s24 = rfl(s24); s30 = rfl(s30); s31 = rfl(s31);
    u32x4 q1, ta; u32x16 tb; u32x2 q2; f32x16 o; u64 t0, t2, t4, t6, t8; unsigned xs = 0, bad = 0;
    asm volatile(
    "s_cmp_lt_u32 s19, 4\n"
    "s_cbranch_scc1 59f\n"
    "v_mbcnt_lo_u32_b32 v22, -1, 0\n"
    "v_mbcnt_hi_u32_b32 v22, -1, v22\n"
    "v_lshrrev_b32 v23, 4, v22\n"
    "v_lshrrev_b32 v24, 3, v22\n"
    "v_lshl_add_u32 v24, s39, 5, v24\n"
    "v_mul_u32_u24 v24, 1280, v24\n"
    "v_xor_b32 v25, v22, v23\n"
    "v_and_b32 v25, 7, v25\n"
    "v_lshl_add_u32 v14, v25, 4, v24\n"
    "v_xor_b32 v15, 64, v14\n"
    "v_add_u32 v16, 18432, v14\n"
    "v_add_u32 v17, 27648, v15\n"
    "v_add_u32 v15, 9216, v15\n"
    "v_lshlrev_b32 v25, 1, v23\n"
    "v_xor_b32 v25, v25, v22\n"
    "v_and_b32 v25, 15, v25\n"
    "v_lshl_add_u32 v23, s39, 4, v23\n"
    "v_mul_u32_u24 v24, 3840, v23\n"
    "v_lshl_add_u32 v18, v25, 4, v24\n"
    "v_xor_b32 v20, 128, v18\n"
    "v_add_u32 v19, 14336, v18\n"
    "v_add_u32 v21, 43008, v20\n"
    "v_add_u32 v20, 28672, v20\n"
    "v_mul_u32_u24 v24, 3072, v23\n"
    "v_lshl_add_u32 v8, v25, 4, v24\n"
    "v_xor_b32 v10, 128, v8\n"
    "v_add_u32 v9, 11264, v8\n"
    "v_add_u32 v11, 33792, v10\n"
    "v_add_u32 v10, 22528, v10\n"
    "s_lshl_b32 s26, s39, 12\n"
    "s_add_u32 s25, s26, 98304\n"
    "s_add_u32 s26, s26, 32768\n"
    "s_mov_b32 s27, 2\n"
    "s_cmp_eq_u32 s35, 15\n"
    "s_cbranch_scc1 11f\n"
    "1:\n"
    "s_cmp_lg_u32 s27, 4\n"
    "s_cbranch_scc1 13f\n"
    "11:\n"
    "v_mbcnt_lo_u32_b32 v140, -1, 0\n"
    "v_mbcnt_hi_u32_b32 v140, -1, v140\n"
    "v_lshrrev_b32 v141, 5, v140\n"
    "v_and_b32 v138, 31, v140\n"
    "v_mad_u32_u24 v135, v141, 20, v138\n"
    "v_lshlrev_b32 v135, 4, v135\n"
    "v_lshlrev_b32 v141, 9, v141\n"
    "v_lshl_add_u32 v134, v138, 3, v141\n"
    "v_and_b32 v139, 15, v140\n"
    "v_lshl_add_u32 v139, v139, 4, v141\n"
    "v_lshrrev_b32 v138, 4, v138\n"
    "v_lshl_add_u32 v139, v138, 3, v139\n"
    "s_mul_i32 s36, s19, 1280\n"
    "s_add_u32 s36, s36, 49152\n"
    "v_add_u32 v139, s36, v139\n"
    "v_mul_u32_u24 v108, 0x5000, v138\n"
    "v_and_b32 v109, 15, v140\n"
    "v_lshl_add_u32 v110, v109, 3, v108\n"
    "v_lshl_add_u32 v108, v109, 4, v108\n"
    "v_mul_u32_u24 v114, 320, v109\n"
    "v_lshrrev_b32 v109, 1, v141\n"
    "v_add_u32 v108, v108, v109\n"
    "v_lshlrev_b32 v109, 4, v140\n"
    "v_lshlrev_b32 v111, 3, v140\n"
    "s_lshr_b32 s18, s19, 2\n"
    "s_mul_i32 s36, s18, 51200\n"
    "v_add_u32 v112, s36, v109\n"
    "s_and_b32 s0, s19, 3\n"
    "s_mul_i32 s0, s0, 5120\n"
    "s_add_u32 s36, s36, s0\n"
    "v_xor_b32 v117, v111, v140\n"
    "v_and_b32 v117, 48, v117\n"
    "v_add3_u32 v114, v114, v117, s36\n"
    "s_lshl_b32 s36, s19, 6\n"
    "v_add_u32 v118, s36, v140\n"
    "v_mul_u32_u24 v117, 0xcccd, v118\n"
    "v_lshrrev_b32 v117, 19, v117\n"
    "v_mul_u32_u24 v116, 10, v117\n"
    "v_sub_u32 v118, v118, v116\n"
    "v_lshlrev_b32 v121, 4, v117\n"
    "v_lshl_add_u32 v119, v118, 4, s11\n"
    "v_mul_u32_u24 v115, 1280, v117\n"
    "s_mul_i32 s36, s10, 81920\n"
    "v_add3_u32 v115, v115, v119, s36\n"
    "v_mul_u32_u24 v116, 320, v117\n"
    "v_lshlrev_b32 v117, 3, v117\n"
    "v_and_b32 v117, 48, v117\n"
    "v_lshlrev_b32 v118, 5, v118\n"
    "v_xor_b32 v118, v118, v117\n"
    "v_add_u32 v116, v116, v118\n"
    "s_lshl_b32 s36, s10, 10\n"
    "s_add_u32 s36, s36, 237576\n"
    "v_add_u32 v118, s36, v121\n"
    "v_and_b32 v143, 7, v140\n"
    "v_lshlrev_b32 v143, 2, v143\n"
    "v_add_u32 v143, 61488, v143\n"
    "s_lshl_b32 s36, s19, 2\n"
    "s_add_u32 s36, s36, 61488\n"
    "v_mov_b32 v142, s36\n"
    "s_and_b32 s36, s19, 3\n"
    "s_lshl2_add_u32 s36, s10, s36\n"
    "s_mul_i32 s36, s36, 20480\n"
    "s_lshl_b32 s0, s18, 11\n"
    "s_lshl2_add_u32 s6, s0, s36\n"
    "s_add_u32 s0, s0, s36\n"
    "s_add_u32 s6, s6, 14684160\n"
    "s_add_u32 s0, s0, 14696448\n"
    "s_add_u32 s6, s40, s6\n"
    "s_addc_u32 s7, s41, 0\n"
    "s_add_u32 s0, s40, s0\n"
    "s_addc_u32 s1, s41, 0\n"
    "s_lshl3_add_u32 s36, s13, s19\n"
    "s_lshr_b32 s8, s36, 1\n"
    "s_mul_i32 s36, s36, 40960\n"
    "s_lshl_b32 s4, s12, 7\n"
    "s_lshl2_add_u32 s2, s4, s36\n"
    "s_lshl_b32 s8, s8, 12\n"
    "s_add_u32 s8, s8, s4\n"
    "s_add_u32 s4, s4, s36\n"
    "s_and_b32 s36, s19, 1\n"
    "s_lshl2_add_u32 s8, s36, s8\n"
    "s_add_u32 s2, s2, 14680064\n"
    "s_add_u32 s4, s4, 14696448\n"
    "s_add_u32 s8, s8, 270336\n"
    "s_add_u32 s2, s40, s2\n"
    "s_addc_u32 s3, s41, 0\n"
    "s_add_u32 s4, s40, s4\n"
    "s_addc_u32 s5, s41, 0\n"
    "s_add_u32 s8, s40, s8\n"
    "s_addc_u32 s9, s41, 0\n"
    "v_add_u32 v136, 133120, v134\n"
    "v_add_u32 v137, 133120, v135\n"
    "v_mov_b32 v138, 0xdddd\n"
    "v_mov_b32 v132, 0\n"
    "v_mov_b32 v133, 0\n"
    "v_mov_b32 v88, 0x3f803f80\n"
    "v_mov_b32 v89, 0\n"
    "v_mov_b32 v90, 0x3f803f80\n"
    "v_mov_b32 v91, 0\n"
    "v_mov_b32 v92, 0\n"
    "s_mov_b32 exec_lo, 0x11000000\n"
    "s_mov_b32 exec_hi, 0x11000000\n"
    "v_mov_b32 v92, 0x3f803f80\n"
    "s_mov_b64 exec, -1\n"
    "v_mov_b32 v93, v92\n"
    "v_mov_b32 v94, v92\n"
    "v_mov_b32 v95, v92\n"
    "v_mov_b32 v96, v92\n"
    "v_mov_b32 v97, v92\n"
    "v_mov_b32 v98, v92\n"
    "v_mov_b32 v99, v92\n"
    "v_mov_b32 v100, v92\n"
    "v_mov_b32 v101, v92\n"
    "v_mov_b32 v102, v92\n"
    "v_mov_b32 v103, v92\n"
    "v_mov_b32 v104, v92\n"
    "v_mov_b32 v105, v92\n"
    "v_mov_b32 v106, v92\n"
    "v_mov_b32 v107, v92\n"
    "v_mov_b32 v32, 0\n"
    "v_mov_b32 v33, 0\n"
    "v_mov_b32 v34, 0\n"
    "v_mov_b32 v35, 0\n"
    "v_mov_b32 v36, 0\n"
    "v_mov_b32 v37, 0\n"
    "v_mov_b32 v38, 0\n"
    "v_mov_b32 v39, 0\n"
    "v_mov_b32 v40, 0\n"
    "v_mov_b32 v41, 0\n"
    "v_mov_b32 v42, 0\n"
    "v_mov_b32 v43, 0\n"
    "v_mov_b32 v44, 0\n"
    "v_mov_b32 v45, 0\n"
    "v_mov_b32 v46, 0\n"
    "v_mov_b32 v47, 0\n"
    "v_mov_b32 v140, -1\n"
    "s_cmp_eq_u32 s35, 15\n"
    "s_cbranch_scc1 12f\n"
    "13:\n"
    "s_waitcnt vmcnt(8)\n"
    "s_barrier\n"
    "s_mov_b32 m0, s25\n"
    "s_lshl_b32 s36, s24, 7\n"
    "s_add_u32 s44, s46, s36\n"
    "s_addc_u32 s45, s47, 0\n"
    "global_load_lds_dwordx4 v14, s[44:45] offset:0\n"
    "global_load_lds_dwordx4 v15, s[44:45] offset:1024\n"
    "global_load_lds_dwordx4 v16, s[44:45] offset:2048\n"
    "global_load_lds_dwordx4 v17, s[44:45] offset:3072\n"
    "s_mov_b32 m0, s26\n"
    "s_mul_i32 s36, s24, 245760\n"
    "s_add_u32 s44, s48, s36\n"
    "s_addc_u32 s45, s49, 0\n"
    "global_load_lds_dwordx4 v18, s[44:45] offset:0\n"
    "global_load_lds_dwordx4 v19, s[44:45] offset:1024\n"
    "global_load_lds_dwordx4 v20, s[44:45] offset:2048\n"
    "global_load_lds_dwordx4 v21, s[44:45] offset:3072\n"
    "s_add_u32 s25, s25, 16384\n"
    "s_cmp_ge_u32 s25, 163840\n"
    "s_cselect_b32 s36, 98304, 0\n"
    "s_sub_u32 s25, s25, s36\n"
    "s_add_u32 s26, s26, 16384\n"
    "s_cmp_ge_u32 s26, 49152\n"
    "s_cselect_b32 s36, 49152, 0\n"
    "s_sub_u32 s26, s26, s36\n"
    "s_add_u32 s24, s24, 1\n"
    "s_cmp_ge_u32 s24, 10\n"
    "s_cselect_b32 s36, 10, 0\n"
    "s_sub_u32 s24, s24, s36\n"
    "s_add_u32 s27, s27, 1\n"
    "s_cmp_lt_u32 s27, 10\n"
    "s_cbranch_scc1 1b\n"
    "s_add_u32 s24, s24, 4\n"
    "s_cmp_ge_u32 s24, 10\n"
    "s_cselect_b32 s36, 10, 0\n"
    "s_sub_u32 s24, s24, s36\n"
    "v_mov_b32 v22, -1\n"
    "s_waitcnt vmcnt(8)\n"
    "s_barrier\n"
    "s_cmp_lg_u32 s34, 0\n"
    "s_cbranch_scc1 3f\n"
    "s_waitcnt vmcnt(0)\n"
    "s_barrier\n"
    "s_barrier\n"
    "s_sleep 40\n"
    "12:\n"
    "s_mov_b32 s27, s39\n"
    "v_lshrrev_b32 v22, 4, v109\n"
    "14:\n"
    "s_lshl_b32 s36, s27, 6\n"
    "v_add_u32 v23, s36, v22\n"
    "v_mul_u32_u24 v24, 0xcccd, v23\n"
    "v_lshrrev_b32 v24, 19, v24\n"
    "v_mul_u32_u24 v24, 1120, v24\n"
    "v_lshl_add_u32 v23, v23, 4, v24\n"
    "s_lshl_b32 s36, s27, 10\n"
    "s_add_u32 m0, s36, 65536\n"
    "s_add_u32 s27, s27, 4\n"
    "global_load_lds_dwordx4 v23, s[50:51]\n"
    "s_cmp_lt_u32 s27, s31\n"
    "s_cbranch_scc1 14b\n"
    "s_cmp_lg_u32 s35, 15\n"
    "s_cbranch_scc1 9f\n"
    "s_barrier\n"
    "s_branch 9f\n"
    "3:\n"
    "s_sub_u32 s25, s25, 65536\n"
    "s_mov_b32 s37, s24\n"
    "s_add_u32 s37, s37, 6\n"
    "s_cmp_ge_u32 s37, 10\n"
    "s_cselect_b32 s36, 10, 0\n"
    "s_sub_u32 s37, s37, s36\n"
    "s_mov_b32 m0, s26\n"
    "s_mul_i32 s36, s24, 196608\n"
    "s_add_u32 s44, s50, s36\n"
    "s_addc_u32 s45, s51, 0\n"
    "global_load_lds_dwordx4 v8, s[44:45] offset:0\n"
    "global_load_lds_dwordx4 v9, s[44:45] offset:1024\n"
    "global_load_lds_dwordx4 v10, s[44:45] offset:2048\n"
    "global_load_lds_dwordx4 v11, s[44:45] offset:3072\n"
    "s_add_u32 s26, s26, 16384\n"
    "s_cmp_ge_u32 s26, 49152\n"
    "s_cselect_b32 s36, 49152, 0\n"
    "s_sub_u32 s26, s26, s36\n"
    "s_add_u32 s24, s24, 1\n"
    "s_cmp_ge_u32 s24, 10\n"
    "s_cselect_b32 s36, 10, 0\n"
    "s_sub_u32 s24, s24, s36\n"
    "s_waitcnt vmcnt(4)\n"
    "s_barrier\n"
    "s_mov_b32 m0, s26\n"
    "s_mul_i32 s36, s24, 196608\n"
    "s_add_u32 s44, s50, s36\n"
    "s_addc_u32 s45, s51, 0\n"
    "global_load_lds_dwordx4 v8, s[44:45] offset:0\n"
    "global_load_lds_dwordx4 v9, s[44:45] offset:1024\n"
    "global_load_lds_dwordx4 v10, s[44:45] offset:2048\n"
    "global_load_lds_dwordx4 v11, s[44:45] offset:3072\n"
    "s_add_u32 s26, s26, 16384\n"
    "s_cmp_ge_u32 s26, 49152\n"
    "s_cselect_b32 s36, 49152, 0\n"
    "s_sub_u32 s26, s26, s36\n"
    "s_add_u32 s24, s24, 1\n"
    "s_cmp_ge_u32 s24, 10\n"
    "s_cselect_b32 s36, 10, 0\n"
    "s_sub_u32 s24, s24, s36\n"
    "s_barrier\n"
    "s_waitcnt vmcnt(4)\n"
    "ds_write_b32 v12, v22\n"
    "s_mov_b32 m0, s26\n"
    "s_mul_i32 s36, s24, 196608\n"
    "s_add_u32 s44, s50, s36\n"
    "s_addc_u32 s45, s51, 0\n"
    "global_load_lds_dwordx4 v8, s[44:45] offset:0\n"
    "global_load_lds_dwordx4 v9, s[44:45] offset:1024\n"
    "global_load_lds_dwordx4 v10, s[44:45] offset:2048\n"
    "global_load_lds_dwordx4 v11, s[44:45] offset:3072\n"
    "s_add_u32 s26, s26, 16384\n"
    "s_cmp_ge_u32 s26, 49152\n"
    "s_cselect_b32 s36, 49152, 0\n"
    "s_sub_u32 s26, s26, s36\n"
    "s_add_u32 s24, s24, 1\n"
    "s_cmp_ge_u32 s24, 10\n"
    "s_cselect_b32 s36, 10, 0\n"
    "s_sub_u32 s24, s24, s36\n"
    "s_waitcnt vmcnt(4)\n"
    "s_barrier\n"
    "s_mov_b32 m0, s26\n"
    "s_mul_i32 s36, s24, 196608\n"
    "s_add_u32 s44, s50, s36\n"
    "s_addc_u32 s45, s51, 0\n"
    "global_load_lds_dwordx4 v8, s[44:45] offset:0\n"
    "global_load_lds_dwordx4 v9, s[44:45] offset:1024\n"
    "global_load_lds_dwordx4 v10, s[44:45] offset:2048\n"
    "global_load_lds_dwordx4 v11, s[44:45] offset:3072\n"
    "s_add_u32 s26, s26, 16384\n"
    "s_cmp_ge_u32 s26, 49152\n"
    "s_cselect_b32 s36, 49152, 0\n"
    "s_sub_u32 s26, s26, s36\n"
    "s_add_u32 s24, s24, 1\n"
    "s_cmp_ge_u32 s24, 10\n"
    "s_cselect_b32 s36, 10, 0\n"
    "s_sub_u32 s24, s24, s36\n"
    "s_waitcnt vmcnt(4)\n"
    "s_barrier\n"
    "s_mov_b32 m0, s26\n"
    "s_mul_i32 s36, s24, 196608\n"
    "s_add_u32 s44, s50, s36\n"
    "s_addc_u32 s45, s51, 0\n"
    "global_load_lds_dwordx4 v8, s[44:45] offset:0\n"
    "global_load_lds_dwordx4 v9, s[44:45] offset:1024\n"
    "global_load_lds_dwordx4 v10, s[44:45] offset:2048\n"
    "global_load_lds_dwordx4 v11, s[44:45] offset:3072\n"
    "s_add_u32 s26, s26, 16384\n"
    "s_cmp_ge_u32 s26, 49152\n"
    "s_cselect_b32 s36, 49152, 0\n"
    "s_sub_u32 s26, s26, s36\n"
    "s_add_u32 s24, s24, 1\n"
    "s_cmp_ge_u32 s24, 10\n"
    "s_cselect_b32 s36, 10, 0\n"
    "s_sub_u32 s24, s24, s36\n"
    "s_waitcnt vmcnt(4)\n"
    "s_barrier\n"
    "s_mov_b32 m0, s26\n"
    "s_mul_i32 s36, s24, 196608\n"
    "s_add_u32 s44, s50, s36\n"
    "s_addc_u32 s45, s51, 0\n"
    "global_load_lds_dwordx4 v8, s[44:45] offset:0\n"
    "global_load_lds_dwordx4 v9, s[44:45] offset:1024\n"
    "global_load_lds_dwordx4 v10, s[44:45] offset:2048\n"
    "global_load_lds_dwordx4 v11, s[44:45] offset:3072\n"
    "s_add_u32 s26, s26, 16384\n"
    "s_cmp_ge_u32 s26, 49152\n"
    "s_cselect_b32 s36, 49152, 0\n"
    "s_sub_u32 s26, s26, s36\n"
    "s_add_u32 s24, s24, 1\n"
    "s_cmp_ge_u32 s24, 10\n"
    "s_cselect_b32 s36, 10, 0\n"
    "s_sub_u32 s24, s24, s36\n"
    "s_mov_b32 m0, s25\n"
    "s_lshl_b32 s36, s37, 7\n"
    "s_add_u32 s44, s46, s36\n"
    "s_addc_u32 s45, s47, 0\n"
    "global_load_lds_dwordx4 v14, s[44:45] offset:0\n"
    "global_load_lds_dwordx4 v15, s[44:45] offset:1024\n"
    "global_load_lds_dwordx4 v16, s[44:45] offset:2048\n"
    "global_load_lds_dwordx4 v17, s[44:45] offset:3072\n"
    "s_add_u32 s25, s25, 16384\n"
    "s_cmp_ge_u32 s25, 163840\n"
    "s_cselect_b32 s36, 98304, 0\n"
    "s_sub_u32 s25, s25, s36\n"
    "s_add_u32 s37, s37, 1\n"
    "s_cmp_ge_u32 s37, 10\n"
    "s_cselect_b32 s36, 10, 0\n"
    "s_sub_u32 s37, s37, s36\n"
    "s_waitcnt vmcnt(8)\n"
    "s_barrier\n"
    "s_mov_b32 m0, s26\n"
    "s_mul_i32 s36, s24, 196608\n"
    "s_add_u32 s44, s50, s36\n"
    "s_addc_u32 s45, s51, 0\n"
    "global_load_lds_dwordx4 v8, s[44:45] offset:0\n"
    "global_load_lds_dwordx4 v9, s[44:45] offset:1024\n"
    "global_load_lds_dwordx4 v10, s[44:45] offset:2048\n"
    "global_load_lds_dwordx4 v11, s[44:45] offset:3072\n"
    "s_add_u32 s26, s26, 16384\n"
    "s_cmp_ge_u32 s26, 49152\n"
    "s_cselect_b32 s36, 49152, 0\n"
    "s_sub_u32 s26, s26, s36\n"
    "s_add_u32 s24, s24, 1\n"
    "s_cmp_ge_u32 s24, 10\n"
    "s_cselect_b32 s36, 10, 0\n"
    "s_sub_u32 s24, s24, s36\n"
    "s_mov_b32 m0, s25\n"
    "s_lshl_b32 s36, s37, 7\n"
    "s_add_u32 s44, s46, s36\n"
    "s_addc_u32 s45, s47, 0\n"
    "global_load_lds_dwordx4 v14, s[44:45] offset:0\n"
    "global_load_lds_dwordx4 v15, s[44:45] offset:1024\n"
    "global_load_lds_dwordx4 v16, s[44:45] offset:2048\n"
    "global_load_lds_dwordx4 v17, s[44:45] offset:3072\n"
    "s_add_u32 s25, s25, 16384\n"
    "s_cmp_ge_u32 s25, 163840\n"
    "s_cselect_b32 s36, 98304, 0\n"
    "s_sub_u32 s25, s25, s36\n"
    "s_add_u32 s37, s37, 1\n"
    "s_cmp_ge_u32 s37, 10\n"
    "s_cselect_b32 s36, 10, 0\n"
    "s_sub_u32 s37, s37, s36\n"
    "s_waitcnt vmcnt(12)\n"
    "s_barrier\n"
    "s_mov_b32 m0, s26\n"
    "s_mul_i32 s36, s24, 196608\n"
    "s_add_u32 s44, s50, s36\n"
    "s_addc_u32 s45, s51, 0\n"
    "global_load_lds_dwordx4 v8, s[44:45] offset:0\n"
    "global_load_lds_dwordx4 v9, s[44:45] offset:1024\n"
    "global_load_lds_dwordx4 v10, s[44:45] offset:2048\n"
    "global_load_lds_dwordx4 v11, s[44:45] offset:3072\n"
    "s_add_u32 s26, s26, 16384\n"
    "s_cmp_ge_u32 s26, 49152\n"
    "s_cselect_b32 s36, 49152, 0\n"
    "s_sub_u32 s26, s26, s36\n"
    "s_add_u32 s24, s24, 1\n"
    "s_cmp_ge_u32 s24, 10\n"
    "s_cselect_b32 s36, 10, 0\n"
    "s_sub_u32 s24, s24, s36\n"
    "s_mov_b32 m0, s25\n"
    "s_lshl_b32 s36, s37, 7\n"
    "s_add_u32 s44, s46, s36\n"
    "s_addc_u32 s45, s47, 0\n"
    "global_load_lds_dwordx4 v14, s[44:45] offset:0\n"
    "global_load_lds_dwordx4 v15, s[44:45] offset:1024\n"
    "global_load_lds_dwordx4 v16, s[44:45] offset:2048\n"
    "global_load_lds_dwordx4 v17, s[44:45] offset:3072\n"
    "s_add_u32 s25, s25, 16384\n"
    "s_cmp_ge_u32 s25, 163840\n"
    "s_cselect_b32 s36, 98304, 0\n"
    "s_sub_u32 s25, s25, s36\n"
    "s_add_u32 s37, s37, 1\n"
    "s_cmp_ge_u32 s37, 10\n"
    "s_cselect_b32 s36, 10, 0\n"
    "s_sub_u32 s37, s37, s36\n"
    "s_waitcnt vmcnt(12)\n"
    "s_barrier\n"
    "s_mov_b32 m0, s26\n"
    "s_mul_i32 s36, s24, 196608\n"
    "s_add_u32 s44, s50, s36\n"
    "s_addc_u32 s45, s51, 0\n"
    "global_load_lds_dwordx4 v8, s[44:45] offset:0\n"
    "global_load_lds_dwordx4 v9, s[44:45] offset:1024\n"
    "global_load_lds_dwordx4 v10, s[44:45] offset:2048\n"
    "global_load_lds_dwordx4 v11, s[44:45] offset:3072\n"
    "s_add_u32 s26, s26, 16384\n"
    "s_cmp_ge_u32 s26, 49152\n"
    "s_cselect_b32 s36, 49152, 0\n"
    "s_sub_u32 s26, s26, s36\n"
    "s_add_u32 s24, s24, 1\n"
    "s_cmp_ge_u32 s24, 10\n"
    "s_cselect_b32 s36, 10, 0\n"
    "s_sub_u32 s24, s24, s36\n"
    "s_mov_b32 m0, s25\n"
    "s_lshl_b32 s36, s37, 7\n"
    "s_add_u32 s44, s46, s36\n"
    "s_addc_u32 s45, s47, 0\n"
    "global_load_lds_dwordx4 v14, s[44:45] offset:0\n"
    "global_load_lds_dwordx4 v15, s[44:45] offset:1024\n"
    "global_load_lds_dwordx4 v16, s[44:45] offset:2048\n"
    "global_load_lds_dwordx4 v17, s[44:45] offset:3072\n"
    "s_add_u32 s25, s25, 16384\n"
    "s_cmp_ge_u32 s25, 163840\n"
    "s_cselect_b32 s36, 98304, 0\n"
    "s_sub_u32 s25, s25, s36\n"
    "s_add_u32 s37, s37, 1\n"
    "s_cmp_ge_u32 s37, 10\n"
    "s_cselect_b32 s36, 10, 0\n"
    "s_sub_u32 s37, s37, s36\n"
    "s_waitcnt vmcnt(12)\n"
    "s_barrier\n"
    "s_mov_b32 m0, s26\n"
    "s_mul_i32 s36, s24, 196608\n"
    "s_add_u32 s44, s50, s36\n"
    "s_addc_u32 s45, s51, 0\n"
    "global_load_lds_dwordx4 v8, s[44:45] offset:0\n"
    "global_load_lds_dwordx4 v9, s[44:45] offset:1024\n"
    "global_load_lds_dwordx4 v10, s[44:45] offset:2048\n"
    "global_load_lds_dwordx4 v11, s[44:45] offset:3072\n"
    "s_add_u32 s26, s26, 16384\n"
    "s_cmp_ge_u32 s26, 49152\n"
    "s_cselect_b32 s36, 49152, 0\n"
    "s_sub_u32 s26, s26, s36\n"
    "s_add_u32 s24, s24, 1\n"
    "s_cmp_ge_u32 s24, 10\n"
    "s_cselect_b32 s36, 10, 0\n"
    "s_sub_u32 s24, s24, s36\n"
    "s_waitcnt vmcnt(8)\n"
    "s_barrier\n"
    "s_waitcnt vmcnt(0)\n"
    "s_barrier\n"
    "9:\n"
    "s_mov_b32 s23, 0\n"
    "s_branch 60f\n"
    "59:\n"
    "s_setprio 3\n"
    "s_cmp_eq_u32 s35, 15\n"
    "s_cbranch_scc1 9f\n"
    "s_cmp_ge_u32 s35, 10\n"
    "s_cbranch_scc1 8f\n"
    "v_mov_b32 v160, 0x3f803f80\n"
    "v_mov_b32 v161, 0x3f803f80\n"
    "v_mov_b32 v162, 0x3f803f80\n"
    "v_mov_b32 v163, 0x3f803f80\n"
    "s_mov_b32 s24, 9\n"
    "s_mov_b32 s28, 16384\n"
    "s_add_u32 s25, s25, 1\n"
    "s_cmp_eq_u32 s25, 6\n"
    "s_cselect_b32 s25, 0, s25\n"
    "s_cselect_b32 s26, 0xfffec000, s28\n"
    "s_add_u32 s29, s29, 1\n"
    "s_cmp_eq_u32 s29, 3\n"
    "s_cselect_b32 s29, 0, s29\n"
    "s_cselect_b32 s27, 0xffff8000, s28\n"
    "s_waitcnt lgkmcnt(0)\n"
    "s_barrier\n"
    "ds_read_b128 v[96:99], v14 offset:0\n"
    "ds_read_b64_tr_b16 v[112:113], v18 offset:0\n"
    "ds_read_b64_tr_b16 v[114:115], v18 offset:1024\n"
    "ds_read_b128 v[100:103], v14 offset:2048\n"
    "ds_read_b64_tr_b16 v[116:117], v19 offset:0\n"
    "ds_read_b64_tr_b16 v[118:119], v19 offset:1024\n"
    "ds_read_b128 v[104:107], v15 offset:0\n"
    "ds_read_b64_tr_b16 v[120:121], v20 offset:0\n"
    "ds_read_b64_tr_b16 v[122:123], v20 offset:1024\n"
    "ds_read_b128 v[108:111], v15 offset:2048\n"
    "ds_read_b64_tr_b16 v[124:125], v21 offset:0\n"
    "ds_read_b64_tr_b16 v[126:127], v21 offset:1024\n"
    "s_waitcnt lgkmcnt(6)\n"
    "v_mfma_f32_16x16x32_bf16 v[32:35], v[112:115], v[96:99], 0\n"
    "ds_read_b128 v[128:131], v16 offset:0\n"
    "v_mfma_f32_16x16x32_bf16 v[36:39], v[112:115], v[100:103], 0\n"
    "ds_read_b64_tr_b16 v[144:145], v18 offset:8192\n"
    "v_mfma_f32_16x16x32_bf16 v[48:51], v[116:119], v[96:99], 0\n"
    "ds_read_b64_tr_b16 v[146:147], v18 offset:9216\n"
    "v_mfma_f32_16x16x32_bf16 v[52:55], v[116:119], v[100:103], 0\n"
    "ds_read_b128 v[132:135], v16 offset:2048\n"
    "v_mfma_f32_16x16x32_bf16 v[22:25], v[160:163], v[112:115], 0\n"
    "ds_read_b64_tr_b16 v[148:149], v19 offset:8192\n"
    "v_mfma_f32_16x16x32_bf16 v[26:29], v[160:163], v[116:119], 0\n"
    "ds_read_b64_tr_b16 v[150:151], v19 offset:9216\n"
    "s_waitcnt lgkmcnt(9)\n"
    "v_mfma_f32_16x16x32_bf16 v[40:43], v[112:115], v[104:107], 0\n"
    "ds_read_b128 v[136:139], v17 offset:0\n"
    "v_mfma_f32_16x16x32_bf16 v[56:59], v[116:119], v[104:107], 0\n"
    "ds_read_b64_tr_b16 v[152:153], v20 offset:8192\n"
    "v_mfma_f32_16x16x32_bf16 v[64:67], v[120:123], v[96:99], 0\n"
    "ds_read_b64_tr_b16 v[154:155], v20 offset:9216\n"
    "v_mfma_f32_16x16x32_bf16 v[68:71], v[120:123], v[100:103], 0\n"
    "ds_read_b128 v[140:143], v17 offset:2048\n"
    "v_mfma_f32_16x16x32_bf16 v[72:75], v[120:123], v[104:107], 0\n"
    "ds_read_b64_tr_b16 v[156:157], v21 offset:8192\n"
    "s_waitcnt lgkmcnt(11)\n"
    "v_mfma_f32_16x16x32_bf16 v[44:47], v[112:115], v[108:111], 0\n"
    "ds_read_b64_tr_b16 v[158:159], v21 offset:9216\n"
    "v_mfma_f32_16x16x32_bf16 v[60:63], v[116:119], v[108:111], 0\n"
    "v_add_u32 v14, s26, v14\n"
    "v_add_u32 v15, s26, v15\n"
    "v_mfma_f32_16x16x32_bf16 v[76:79], v[120:123], v[108:111], 0\n"
    "v_add_u32 v16, s26, v16\n"
    "v_add_u32 v17, s26, v17\n"
    "v_mfma_f32_16x16x32_bf16 v[80:83], v[124:127], v[96:99], 0\n"
    "v_add_u32 v18, s27, v18\n"
    "v_add_u32 v19, s27, v19\n"
    "v_mfma_f32_16x16x32_bf16 v[84:87], v[124:127], v[100:103], 0\n"
    "v_add_u32 v20, s27, v20\n"
    "v_add_u32 v21, s27, v21\n"
    "v_mfma_f32_16x16x32_bf16 v[88:91], v[124:127], v[104:107], 0\n"
    "v_mfma_f32_16x16x32_bf16 v[92:95], v[124:127], v[108:111], 0\n"
    "1:\n"
    "s_waitcnt lgkmcnt(0)\n"
    "s_barrier\n"
    "v_mfma_f32_16x16x32_bf16 v[32:35], v[144:147], v[128:131], v[32:35]\n"
    "ds_read_b128 v[96:99], v14 offset:0\n"
    "v_mfma_f32_16x16x32_bf16 v[36:39], v[144:147], v[132:135], v[36:39]\n"
    "ds_read_b64_tr_b16 v[112:113], v18 offset:0\n"
    "v_mfma_f32_16x16x32_bf16 v[48:51], v[148:151], v[128:131], v[48:51]\n"
    "ds_read_b64_tr_b16 v[114:115], v18 offset:1024\n"
    "v_mfma_f32_16x16x32_bf16 v[52:55], v[148:151], v[132:135], v[52:55]\n"
    "ds_read_b128 v[100:103], v14 offset:2048\n"
    "v_mfma_f32_16x16x32_bf16 v[22:25], v[160:163], v[144:147], v[22:25]\n"
    "ds_read_b64_tr_b16 v[116:117], v19 offset:0\n"
    "v_mfma_f32_16x16x32_bf16 v[26:29], v[160:163], v[148:151], v[26:29]\n"
    "ds_read_b64_tr_b16 v[118:119], v19 offset:1024\n"
    "v_mfma_f32_16x16x32_bf16 v[40:43], v[144:147], v[136:139], v[40:43]\n"
    "ds_read_b128 v[104:107], v15 offset:0\n"
    "v_mfma_f32_16x16x32_bf16 v[56:59], v[148:151], v[136:139], v[56:59]\n"
    "ds_read_b64_tr_b16 v[120:121], v20 offset:0\n"
    "v_mfma_f32_16x16x32_bf16 v[64:67], v[152:155], v[128:131], v[64:67]\n"
    "ds_read_b64_tr_b16 v[122:123], v20 offset:1024\n"
    "v_mfma_f32_16x16x32_bf16 v[68:71], v[152:155], v[132:135], v[68:71]\n"
    "ds_read_b128 v[108:111], v15 offset:2048\n"
    "v_mfma_f32_16x16x32_bf16 v[72:75], v[152:155], v[136:139], v[72:75]\n"
    "ds_read_b64_tr_b16 v[124:125], v21 offset:0\n"
    "v_mfma_f32_16x16x32_bf16 v[44:47], v[144:147], v[140:143], v[44:47]\n"
    "ds_read_b64_tr_b16 v[126:127], v21 offset:1024\n"
    "v_mfma_f32_16x16x32_bf16 v[60:63], v[148:151], v[140:143], v[60:63]\n"
    "s_add_u32 s25, s25, 1\n"
    "s_cmp_eq_u32 s25, 6\n"
    "v_mfma_f32_16x16x32_bf16 v[76:79], v[152:155], v[140:143], v[76:79]\n"
    "s_cselect_b32 s25, 0, s25\n"
    "s_cselect_b32 s26, 0xfffec000, s28\n"
    "v_mfma_f32_16x16x32_bf16 v[80:83], v[156:159], v[128:131], v[80:83]\n"
    "s_add_u32 s29, s29, 1\n"
    "s_cmp_eq_u32 s29, 3\n"
    "v_mfma_f32_16x16x32_bf16 v[84:87], v[156:159], v[132:135], v[84:87]\n"
    "s_cselect_b32 s29, 0, s29\n"
    "s_cselect_b32 s27, 0xffff8000, s28\n"
    "v_mfma_f32_16x16x32_bf16 v[88:91], v[156:159], v[136:139], v[88:91]\n"
    "s_sub_u32 s24, s24, 1\n"
    "v_mfma_f32_16x16x32_bf16 v[92:95], v[156:159], v[140:143], v[92:95]\n"
    "s_waitcnt lgkmcnt(6)\n"
    "v_mfma_f32_16x16x32_bf16 v[32:35], v[112:115], v[96:99], v[32:35]\n"
    "ds_read_b128 v[128:131], v16 offset:0\n"
    "v_mfma_f32_16x16x32_bf16 v[36:39], v[112:115], v[100:103], v[36:39]\n"
    "ds_read_b64_tr_b16 v[144:145], v18 offset:8192\n"
    "v_mfma_f32_16x16x32_bf16 v[48:51], v[116:119], v[96:99], v[48:51]\n"
    "ds_read_b64_tr_b16 v[146:147], v18 offset:9216\n"
    "v_mfma_f32_16x16x32_bf16 v[52:55], v[116:119], v[100:103], v[52:55]\n"
    "ds_read_b128 v[132:135], v16 offset:2048\n"
    "v_mfma_f32_16x16x32_bf16 v[22:25], v[160:163], v[112:115], v[22:25]\n"
    "ds_read_b64_tr_b16 v[148:149], v19 offset:8192\n"
    "v_mfma_f32_16x16x32_bf16 v[26:29], v[160:163], v[116:119], v[26:29]\n"
    "ds_read_b64_tr_b16 v[150:151], v19 offset:9216\n"
    "s_waitcnt lgkmcnt(9)\n"
    "v_mfma_f32_16x16x32_bf16 v[40:43], v[112:115], v[104:107], v[40:43]\n"
    "ds_read_b128 v[136:139], v17 offset:0\n"
    "v_mfma_f32_16x16x32_bf16 v[56:59], v[116:119], v[104:107], v[56:59]\n"
    "ds_read_b64_tr_b16 v[152:153], v20 offset:8192\n"
    "v_mfma_f32_16x16x32_bf16 v[64:67], v[120:123], v[96:99], v[64:67]\n"
    "ds_read_b64_tr_b16 v[154:155], v20 offset:9216\n"
    "v_mfma_f32_16x16x32_bf16 v[68:71], v[120:123], v[100:103], v[68:71]\n"
    "ds_read_b128 v[140:143], v17 offset:2048\n"
    "v_mfma_f32_16x16x32_bf16 v[72:75], v[120:123], v[104:107], v[72:75]\n"
    "ds_read_b64_tr_b16 v[156:157], v21 offset:8192\n"
    "s_waitcnt lgkmcnt(11)\n"
    "v_mfma_f32_16x16x32_bf16 v[44:47], v[112:115], v[108:111], v[44:47]\n"
    "ds_read_b64_tr_b16 v[158:159], v21 offset:9216\n"
    "v_mfma_f32_16x16x32_bf16 v[60:63], v[116:119], v[108:111], v[60:63]\n"
    "v_add_u32 v14, s26, v14\n"
    "v_add_u32 v15, s26, v15\n"
    "v_mfma_f32_16x16x32_bf16 v[76:79], v[120:123], v[108:111], v[76:79]\n"
    "v_add_u32 v16, s26, v16\n"
    "v_add_u32 v17, s26, v17\n"
    "v_mfma_f32_16x16x32_bf16 v[80:83], v[124:127], v[96:99], v[80:83]\n"
    "v_add_u32 v18, s27, v18\n"
    "v_add_u32 v19, s27, v19\n"
    "v_mfma_f32_16x16x32_bf16 v[84:87], v[124:127], v[100:103], v[84:87]\n"
    "v_add_u32 v20, s27, v20\n"
    "v_add_u32 v21, s27, v21\n"
    "v_mfma_f32_16x16x32_bf16 v[88:91], v[124:127], v[104:107], v[88:91]\n"
    "s_cmp_lg_u32 s24, 0\n"
    "v_mfma_f32_16x16x32_bf16 v[92:95], v[124:127], v[108:111], v[92:95]\n"
    "s_cbranch_scc1 1b\n"
    "s_waitcnt lgkmcnt(0)\n"
    "v_mfma_f32_16x16x32_bf16 v[22:25], v[160:163], v[144:147], v[22:25]\n"
    "v_mfma_f32_16x16x32_bf16 v[26:29], v[160:163], v[148:151], v[26:29]\n"
    "v_mfma_f32_16x16x32_bf16 v[32:35], v[144:147], v[128:131], v[32:35]\n"
    "v_mfma_f32_16x16x32_bf16 v[36:39], v[144:147], v[132:135], v[36:39]\n"
    "v_mfma_f32_16x16x32_bf16 v[48:51], v[148:151], v[128:131], v[48:51]\n"
    "s_mov_b64 exec, 0xffff\n"
    "s_nop 3\n"
    "ds_write_b32 v30, v22\n"
    "ds_write_b32 v30, v26 offset:64\n"
    "s_mov_b64 exec, -1\n"
    "v_mfma_f32_16x16x32_bf16 v[52:55], v[148:151], v[132:135], v[52:55]\n"
    "v_mfma_f32_16x16x32_bf16 v[40:43], v[144:147], v[136:139], v[40:43]\n"
    "v_mfma_f32_16x16x32_bf16 v[56:59], v[148:151], v[136:139], v[56:59]\n"
    "s_waitcnt lgkmcnt(0)\n"
    "s_barrier\n"
    "v_mfma_f32_16x16x32_bf16 v[64:67], v[152:155], v[128:131], v[64:67]\n"
    "ds_read2_b32 v[112:113], v8 offset0:0 offset1:128\n"
    "ds_read2_b32 v[114:115], v8 offset0:16 offset1:144\n"
    "v_mfma_f32_16x16x32_bf16 v[68:71], v[152:155], v[132:135], v[68:71]\n"
    "ds_read2_b32 v[116:117], v9 offset0:0 offset1:128\n"
    "ds_read2_b32 v[118:119], v9 offset0:16 offset1:144\n"
    "v_mfma_f32_16x16x32_bf16 v[72:75], v[152:155], v[136:139], v[72:75]\n"
    "ds_read_b128 v[96:99], v10 offset:0\n"
    "ds_read_b128 v[100:103], v10 offset:64\n"
    "v_mfma_f32_16x16x32_bf16 v[44:47], v[144:147], v[140:143], v[44:47]\n"
    "ds_read_b128 v[104:107], v11 offset:0\n"
    "ds_read_b128 v[108:111], v11 offset:64\n"
    "v_mfma_f32_16x16x32_bf16 v[60:63], v[148:151], v[140:143], v[60:63]\n"
    "ds_read_b128 v[26:29], v12\n"
    "v_mfma_f32_16x16x32_bf16 v[76:79], v[152:155], v[140:143], v[76:79]\n"
    "v_mfma_f32_16x16x32_bf16 v[80:83], v[156:159], v[128:131], v[80:83]\n"
    "v_mfma_f32_16x16x32_bf16 v[84:87], v[156:159], v[132:135], v[84:87]\n"
    "v_mfma_f32_16x16x32_bf16 v[88:91], v[156:159], v[136:139], v[88:91]\n"
    "v_mfma_f32_16x16x32_bf16 v[92:95], v[156:159], v[140:143], v[92:95]\n"
    "s_waitcnt lgkmcnt(0)\n"
    "s_setprio 3\n"
    "v_mul_f32 v113, s30, v113\n"
    "v_mul_f32 v115, s30, v115\n"
    "v_mul_f32 v117, s30, v117\n"
    "v_mul_f32 v119, s30, v119\n"
    "v_add_u32 v120, s31, v26\n"
    "v_add_u32 v121, s31, v27\n"
    "v_add_u32 v122, s31, v28\n"
    "v_add_u32 v123, s31, v29\n"
    "v_pk_fma_f32 v[32:33], v[112:113], v[96:97], v[32:33] op_sel:[0,0,0] op_sel_hi:[0,1,1]\n"
    "v_pk_fma_f32 v[34:35], v[112:113], v[98:99], v[34:35] op_sel:[0,0,0] op_sel_hi:[0,1,1]\n"
    "v_pk_fma_f32 v[36:37], v[114:115], v[96:97], v[36:37] op_sel:[0,0,0] op_sel_hi:[0,1,1]\n"
    "v_pk_fma_f32 v[38:39], v[114:115], v[98:99], v[38:39] op_sel:[0,0,0] op_sel_hi:[0,1,1]\n"
    "v_pk_mul_f32 v[32:33], v[112:113], v[32:33] op_sel:[1,0] op_sel_hi:[1,1]\n"
    "v_pk_mul_f32 v[34:35], v[112:113], v[34:35] op_sel:[1,0] op_sel_hi:[1,1]\n"
    "v_pk_mul_f32 v[36:37], v[114:115], v[36:37] op_sel:[1,0] op_sel_hi:[1,1]\n"
    "v_pk_mul_f32 v[38:39], v[114:115], v[38:39] op_sel:[1,0] op_sel_hi:[1,1]\n"
    "v_cvt_pk_bf16_f32 v128, v32, v33\n"
    "v_cvt_pk_bf16_f32 v129, v34, v35\n"
    "v_cvt_pk_bf16_f32 v130, v36, v37\n"
    "v_cvt_pk_bf16_f32 v131, v38, v39\n"
    "global_store_dwordx4 v26, v[128:131], s[40:41] sc1\n"
    "v_pk_fma_f32 v[48:49], v[112:113], v[100:101], v[48:49] op_sel:[0,0,0] op_sel_hi:[0,1,1]\n"
    "v_pk_fma_f32 v[50:51], v[112:113], v[102:103], v[50:51] op_sel:[0,0,0] op_sel_hi:[0,1,1]\n"
    "v_pk_fma_f32 v[52:53], v[114:115], v[100:101], v[52:53] op_sel:[0,0,0] op_sel_hi:[0,1,1]\n"
    "v_pk_fma_f32 v[54:55], v[114:115], v[102:103], v[54:55] op_sel:[0,0,0] op_sel_hi:[0,1,1]\n"
    "v_pk_mul_f32 v[48:49], v[112:113], v[48:49] op_sel:[1,0] op_sel_hi:[1,1]\n"
    "v_pk_mul_f32 v[50:51], v[112:113], v[50:51] op_sel:[1,0] op_sel_hi:[1,1]\n"
    "v_pk_mul_f32 v[52:53], v[114:115], v[52:53] op_sel:[1,0] op_sel_hi:[1,1]\n"
    "v_pk_mul_f32 v[54:55], v[114:115], v[54:55] op_sel:[1,0] op_sel_hi:[1,1]\n"
    "v_cvt_pk_bf16_f32 v132, v48, v49\n"
    "v_cvt_pk_bf16_f32 v133, v50, v51\n"
    "v_cvt_pk_bf16_f32 v134, v52, v53\n"
    "v_cvt_pk_bf16_f32 v135, v54, v55\n"
    "global_store_dwordx4 v27, v[132:135], s[40:41] sc1\n"
    "v_pk_fma_f32 v[64:65], v[112:113], v[104:105], v[64:65] op_sel:[0,0,0] op_sel_hi:[0,1,1]\n"
    "v_pk_fma_f32 v[66:67], v[112:113], v[106:107], v[66:67] op_sel:[0,0,0] op_sel_hi:[0,1,1]\n"
    "v_pk_fma_f32 v[68:69], v[114:115], v[104:105], v[68:69] op_sel:[0,0,0] op_sel_hi:[0,1,1]\n"
    "v_pk_fma_f32 v[70:71], v[114:115], v[106:107], v[70:71] op_sel:[0,0,0] op_sel_hi:[0,1,1]\n"
    "v_pk_mul_f32 v[64:65], v[112:113], v[64:65] op_sel:[1,0] op_sel_hi:[1,1]\n"
    "v_pk_mul_f32 v[66:67], v[112:113], v[66:67] op_sel:[1,0] op_sel_hi:[1,1]\n"
    "v_pk_mul_f32 v[68:69], v[114:115], v[68:69] op_sel:[1,0] op_sel_hi:[1,1]\n"
    "v_pk_mul_f32 v[70:71], v[114:115], v[70:71] op_sel:[1,0] op_sel_hi:[1,1]\n"
    "v_cvt_pk_bf16_f32 v136, v64, v65\n"
    "v_cvt_pk_bf16_f32 v137, v66, v67\n"
    "v_cvt_pk_bf16_f32 v138, v68, v69\n"
    "v_cvt_pk_bf16_f32 v139, v70, v71\n"
    "global_store_dwordx4 v28, v[136:139], s[40:41] sc1\n"
    "v_pk_fma_f32 v[80:81], v[112:113], v[108:109], v[80:81] op_sel:[0,0,0] op_sel_hi:[0,1,1]\n"
    "v_pk_fma_f32 v[82:83], v[112:113], v[110:111], v[82:83] op_sel:[0,0,0] op_sel_hi:[0,1,1]\n"
    "v_pk_fma_f32 v[84:85], v[114:115], v[108:109], v[84:85] op_sel:[0,0,0] op_sel_hi:[0,1,1]\n"
    "v_pk_fma_f32 v[86:87], v[114:115], v[110:111], v[86:87] op_sel:[0,0,0] op_sel_hi:[0,1,1]\n"
    "v_pk_mul_f32 v[80:81], v[112:113], v[80:81] op_sel:[1,0] op_sel_hi:[1,1]\n"
    "v_pk_mul_f32 v[82:83], v[112:113], v[82:83] op_sel:[1,0] op_sel_hi:[1,1]\n"
    "v_pk_mul_f32 v[84:85], v[114:115], v[84:85] op_sel:[1,0] op_sel_hi:[1,1]\n"
    "v_pk_mul_f32 v[86:87], v[114:115], v[86:87] op_sel:[1,0] op_sel_hi:[1,1]\n"
    "v_cvt_pk_bf16_f32 v140, v80, v81\n"
    "v_cvt_pk_bf16_f32 v141, v82, v83\n"
    "v_cvt_pk_bf16_f32 v142, v84, v85\n"
    "v_cvt_pk_bf16_f32 v143, v86, v87\n"
    "global_store_dwordx4 v29, v[140:143], s[40:41] sc1\n"
    "v_pk_fma_f32 v[40:41], v[116:117], v[96:97], v[40:41] op_sel:[0,0,0] op_sel_hi:[0,1,1]\n"
    "v_pk_fma_f32 v[42:43], v[116:117], v[98:99], v[42:43] op_sel:[0,0,0] op_sel_hi:[0,1,1]\n"
    "v_pk_fma_f32 v[44:45], v[118:119], v[96:97], v[44:45] op_sel:[0,0,0] op_sel_hi:[0,1,1]\n"
    "v_pk_fma_f32 v[46:47], v[118:119], v[98:99], v[46:47] op_sel:[0,0,0] op_sel_hi:[0,1,1]\n"
    "v_pk_mul_f32 v[40:41], v[116:117], v[40:41] op_sel:[1,0] op_sel_hi:[1,1]\n"
    "v_pk_mul_f32 v[42:43], v[116:117], v[42:43] op_sel:[1,0] op_sel_hi:[1,1]\n"
    "v_pk_mul_f32 v[44:45], v[118:119], v[44:45] op_sel:[1,0] op_sel_hi:[1,1]\n"
    "v_pk_mul_f32 v[46:47], v[118:119], v[46:47] op_sel:[1,0] op_sel_hi:[1,1]\n"
    "v_cvt_pk_bf16_f32 v144, v40, v41\n"
    "v_cvt_pk_bf16_f32 v145, v42, v43\n"
    "v_cvt_pk_bf16_f32 v146, v44, v45\n"
    "v_cvt_pk_bf16_f32 v147, v46, v47\n"
    "global_store_dwordx4 v120, v[144:147], s[40:41] sc1\n"
    "v_pk_fma_f32 v[56:57], v[116:117], v[100:101], v[56:57] op_sel:[0,0,0] op_sel_hi:[0,1,1]\n"
    "v_pk_fma_f32 v[58:59], v[116:117], v[102:103], v[58:59] op_sel:[0,0,0] op_sel_hi:[0,1,1]\n"
    "v_pk_fma_f32 v[60:61], v[118:119], v[100:101], v[60:61] op_sel:[0,0,0] op_sel_hi:[0,1,1]\n"
    "v_pk_fma_f32 v[62:63], v[118:119], v[102:103], v[62:63] op_sel:[0,0,0] op_sel_hi:[0,1,1]\n"
    "v_pk_mul_f32 v[56:57], v[116:117], v[56:57] op_sel:[1,0] op_sel_hi:[1,1]\n"
    "v_pk_mul_f32 v[58:59], v[116:117], v[58:59] op_sel:[1,0] op_sel_hi:[1,1]\n"
    "v_pk_mul_f32 v[60:61], v[118:119], v[60:61] op_sel:[1,0] op_sel_hi:[1,1]\n"
    "v_pk_mul_f32 v[62:63], v[118:119], v[62:63] op_sel:[1,0] op_sel_hi:[1,1]\n"
    "v_cvt_pk_bf16_f32 v148, v56, v57\n"
    "v_cvt_pk_bf16_f32 v149, v58, v59\n"
    "v_cvt_pk_bf16_f32 v150, v60, v61\n"
    "v_cvt_pk_bf16_f32 v151, v62, v63\n"
    "global_store_dwordx4 v121, v[148:151], s[40:41] sc1\n"
    "v_pk_fma_f32 v[72:73], v[116:117], v[104:105], v[72:73] op_sel:[0,0,0] op_sel_hi:[0,1,1]\n"
    "v_pk_fma_f32 v[74:75], v[116:117], v[106:107], v[74:75] op_sel:[0,0,0] op_sel_hi:[0,1,1]\n"
    "v_pk_fma_f32 v[76:77], v[118:119], v[104:105], v[76:77] op_sel:[0,0,0] op_sel_hi:[0,1,1]\n"
    "v_pk_fma_f32 v[78:79], v[118:119], v[106:107], v[78:79] op_sel:[0,0,0] op_sel_hi:[0,1,1]\n"
    "v_pk_mul_f32 v[72:73], v[116:117], v[72:73] op_sel:[1,0] op_sel_hi:[1,1]\n"
    "v_pk_mul_f32 v[74:75], v[116:117], v[74:75] op_sel:[1,0] op_sel_hi:[1,1]\n"
    "v_pk_mul_f32 v[76:77], v[118:119], v[76:77] op_sel:[1,0] op_sel_hi:[1,1]\n"
    "v_pk_mul_f32 v[78:79], v[118:119], v[78:79] op_sel:[1,0] op_sel_hi:[1,1]\n"
    "v_cvt_pk_bf16_f32 v152, v72, v73\n"
    "v_cvt_pk_bf16_f32 v153, v74, v75\n"
    "v_cvt_pk_bf16_f32 v154, v76, v77\n"
    "v_cvt_pk_bf16_f32 v155, v78, v79\n"
    "global_store_dwordx4 v122, v[152:155], s[40:41] sc1\n"
    "v_pk_fma_f32 v[88:89], v[116:117], v[108:109], v[88:89] op_sel:[0,0,0] op_sel_hi:[0,1,1]\n"
    "v_pk_fma_f32 v[90:91], v[116:117], v[110:111], v[90:91] op_sel:[0,0,0] op_sel_hi:[0,1,1]\n"
    "v_pk_fma_f32 v[92:93], v[118:119], v[108:109], v[92:93] op_sel:[0,0,0] op_sel_hi:[0,1,1]\n"
    "v_pk_fma_f32 v[94:95], v[118:119], v[110:111], v[94:95] op_sel:[0,0,0] op_sel_hi:[0,1,1]\n"
    "v_pk_mul_f32 v[88:89], v[116:117], v[88:89] op_sel:[1,0] op_sel_hi:[1,1]\n"
    "v_pk_mul_f32 v[90:91], v[116:117], v[90:91] op_sel:[1,0] op_sel_hi:[1,1]\n"
    "v_pk_mul_f32 v[92:93], v[118:119], v[92:93] op_sel:[1,0] op_sel_hi:[1,1]\n"
    "v_pk_mul_f32 v[94:95], v[118:119], v[94:95] op_sel:[1,0] op_sel_hi:[1,1]\n"
    "v_cvt_pk_bf16_f32 v156, v88, v89\n"
    "v_cvt_pk_bf16_f32 v157, v90, v91\n"
    "v_cvt_pk_bf16_f32 v158, v92, v93\n"
    "v_cvt_pk_bf16_f32 v159, v94, v95\n"
    "global_store_dwordx4 v123, v[156:159], s[40:41] sc1\n"
    "s_cmp_eq_u32 s34, 0\n"
    "s_cbranch_scc1 2f\n"
    "s_barrier\n"
    "2:\n"
    "s_nop 1\n"
    "s_branch 9f\n"
    "8:\n"
    "v_mov_b32 v160, 0x3f803f80\n"
    "v_mov_b32 v161, 0x3f803f80\n"
    "v_mov_b32 v162, 0x3f803f80\n"
    "v_mov_b32 v163, 0x3f803f80\n"
    "s_mov_b32 s24, 9\n"
    "s_mov_b32 s28, 16384\n"
    "s_add_u32 s25, s25, 1\n"
    "s_cmp_eq_u32 s25, 6\n"
    "s_cselect_b32 s25, 0, s25\n"
    "s_cselect_b32 s26, 0xfffec000, s28\n"
    "s_add_u32 s29, s29, 1\n"
    "s_cmp_eq_u32 s29, 3\n"
    "s_cselect_b32 s29, 0, s29\n"
    "s_cselect_b32 s27, 0xffff8000, s28\n"
    "s_waitcnt lgkmcnt(0)\n"
    "s_barrier\n"
    "ds_read_b128 v[96:99], v14 offset:0\n"
    "ds_read_b64_tr_b16 v[112:113], v18 offset:0\n"
    "ds_read_b64_tr_b16 v[114:115], v18 offset:1024\n"
    "ds_read_b128 v[100:103], v14 offset:2048\n"
    "ds_read_b64_tr_b16 v[116:117], v19 offset:0\n"
    "ds_read_b64_tr_b16 v[118:119], v19 offset:1024\n"
    "ds_read_b128 v[104:107], v15 offset:0\n"
    "ds_read_b64_tr_b16 v[120:121], v20 offset:0\n"
    "ds_read_b64_tr_b16 v[122:123], v20 offset:1024\n"
    "ds_read_b128 v[108:111], v15 offset:2048\n"
    "ds_read_b64_tr_b16 v[124:125], v21 offset:0\n"
    "ds_read_b64_tr_b16 v[126:127], v21 offset:1024\n"
    "s_waitcnt lgkmcnt(6)\n"
    "v_mfma_f32_16x16x32_bf16 v[32:35], v[96:99], v[112:115], 0\n"
    "ds_read_b128 v[128:131], v16 offset:0\n"
    "v_mfma_f32_16x16x32_bf16 v[36:39], v[100:103], v[112:115], 0\n"
    "ds_read_b64_tr_b16 v[144:145], v18 offset:8192\n"
    "v_mfma_f32_16x16x32_bf16 v[48:51], v[96:99], v[116:119], 0\n"
    "ds_read_b64_tr_b16 v[146:147], v18 offset:9216\n"
    "v_mfma_f32_16x16x32_bf16 v[52:55], v[100:103], v[116:119], 0\n"
    "ds_read_b128 v[132:135], v16 offset:2048\n"
    "v_mfma_f32_16x16x32_bf16 v[22:25], v[160:163], v[112:115], 0\n"
    "ds_read_b64_tr_b16 v[148:149], v19 offset:8192\n"
    "v_mfma_f32_16x16x32_bf16 v[26:29], v[160:163], v[116:119], 0\n"
    "ds_read_b64_tr_b16 v[150:151], v19 offset:9216\n"
    "s_waitcnt lgkmcnt(9)\n"
    "v_mfma_f32_16x16x32_bf16 v[40:43], v[104:107], v[112:115], 0\n"
    "ds_read_b128 v[136:139], v17 offset:0\n"
    "v_mfma_f32_16x16x32_bf16 v[56:59], v[104:107], v[116:119], 0\n"
    "ds_read_b64_tr_b16 v[152:153], v20 offset:8192\n"
    "v_mfma_f32_16x16x32_bf16 v[64:67], v[96:99], v[120:123], 0\n"
    "ds_read_b64_tr_b16 v[154:155], v20 offset:9216\n"
    "v_mfma_f32_16x16x32_bf16 v[68:71], v[100:103], v[120:123], 0\n"
    "ds_read_b128 v[140:143], v17 offset:2048\n"
    "v_mfma_f32_16x16x32_bf16 v[72:75], v[104:107], v[120:123], 0\n"
    "ds_read_b64_tr_b16 v[156:157], v21 offset:8192\n"
    "s_waitcnt lgkmcnt(11)\n"
    "v_mfma_f32_16x16x32_bf16 v[44:47], v[108:111], v[112:115], 0\n"
    "ds_read_b64_tr_b16 v[158:159], v21 offset:9216\n"
    "v_mfma_f32_16x16x32_bf16 v[60:63], v[108:111], v[116:119], 0\n"
    "v_add_u32 v14, s26, v14\n"
    "v_add_u32 v15, s26, v15\n"
    "v_mfma_f32_16x16x32_bf16 v[76:79], v[108:111], v[120:123], 0\n"
    "v_add_u32 v16, s26, v16\n"
    "v_add_u32 v17, s26, v17\n"
    "v_mfma_f32_16x16x32_bf16 v[80:83], v[96:99], v[124:127], 0\n"
    "v_add_u32 v18, s27, v18\n"
    "v_add_u32 v19, s27, v19\n"
    "v_mfma_f32_16x16x32_bf16 v[84:87], v[100:103], v[124:127], 0\n"
    "v_add_u32 v20, s27, v20\n"
    "v_add_u32 v21, s27, v21\n"
    "v_mfma_f32_16x16x32_bf16 v[88:91], v[104:107], v[124:127], 0\n"
    "v_mfma_f32_16x16x32_bf16 v[92:95], v[108:111], v[124:127], 0\n"
    "1:\n"
    "s_waitcnt lgkmcnt(0)\n"
    "s_barrier\n"
    "v_mfma_f32_16x16x32_bf16 v[32:35], v[128:131], v[144:147], v[32:35]\n"
    "ds_read_b128 v[96:99], v14 offset:0\n"
    "v_mfma_f32_16x16x32_bf16 v[36:39], v[132:135], v[144:147], v[36:39]\n"
    "ds_read_b64_tr_b16 v[112:113], v18 offset:0\n"
    "v_mfma_f32_16x16x32_bf16 v[48:51], v[128:131], v[148:151], v[48:51]\n"
    "ds_read_b64_tr_b16 v[114:115], v18 offset:1024\n"
    "v_mfma_f32_16x16x32_bf16 v[52:55], v[132:135], v[148:151], v[52:55]\n"
    "ds_read_b128 v[100:103], v14 offset:2048\n"
    "v_mfma_f32_16x16x32_bf16 v[22:25], v[160:163], v[144:147], v[22:25]\n"
    "ds_read_b64_tr_b16 v[116:117], v19 offset:0\n"
    "v_mfma_f32_16x16x32_bf16 v[26:29], v[160:163], v[148:151], v[26:29]\n"
    "ds_read_b64_tr_b16 v[118:119], v19 offset:1024\n"
    "v_mfma_f32_16x16x32_bf16 v[40:43], v[136:139], v[144:147], v[40:43]\n"
    "ds_read_b128 v[104:107], v15 offset:0\n"
    "v_mfma_f32_16x16x32_bf16 v[56:59], v[136:139], v[148:151], v[56:59]\n"
    "ds_read_b64_tr_b16 v[120:121], v20 offset:0\n"
    "v_mfma_f32_16x16x32_bf16 v[64:67], v[128:131], v[152:155], v[64:67]\n"
    "ds_read_b64_tr_b16 v[122:123], v20 offset:1024\n"
    "v_mfma_f32_16x16x32_bf16 v[68:71], v[132:135], v[152:155], v[68:71]\n"
    "ds_read_b128 v[108:111], v15 offset:2048\n"
    "v_mfma_f32_16x16x32_bf16 v[72:75], v[136:139], v[152:155], v[72:75]\n"
    "ds_read_b64_tr_b16 v[124:125], v21 offset:0\n"
    "v_mfma_f32_16x16x32_bf16 v[44:47], v[140:143], v[144:147], v[44:47]\n"
    "ds_read_b64_tr_b16 v[126:127], v21 offset:1024\n"
    "v_mfma_f32_16x16x32_bf16 v[60:63], v[140:143], v[148:151], v[60:63]\n"
    "s_add_u32 s25, s25, 1\n"
    "s_cmp_eq_u32 s25, 6\n"
    "v_mfma_f32_16x16x32_bf16 v[76:79], v[140:143], v[152:155], v[76:79]\n"
    "s_cselect_b32 s25, 0, s25\n"
    "s_cselect_b32 s26, 0xfffec000, s28\n"
    "v_mfma_f32_16x16x32_bf16 v[80:83], v[128:131], v[156:159], v[80:83]\n"
    "s_add_u32 s29, s29, 1\n"
    "s_cmp_eq_u32 s29, 3\n"
    "v_mfma_f32_16x16x32_bf16 v[84:87], v[132:135], v[156:159], v[84:87]\n"
    "s_cselect_b32 s29, 0, s29\n"
    "s_cselect_b32 s27, 0xffff8000, s28\n"
    "v_mfma_f32_16x16x32_bf16 v[88:91], v[136:139], v[156:159], v[88:91]\n"
    "s_sub_u32 s24, s24, 1\n"
    "v_mfma_f32_16x16x32_bf16 v[92:95], v[140:143], v[156:159], v[92:95]\n"
    "s_waitcnt lgkmcnt(6)\n"
    "v_mfma_f32_16x16x32_bf16 v[32:35], v[96:99], v[112:115], v[32:35]\n"
    "ds_read_b128 v[128:131], v16 offset:0\n"
    "v_mfma_f32_16x16x32_bf16 v[36:39], v[100:103], v[112:115], v[36:39]\n"
    "ds_read_b64_tr_b16 v[144:145], v18 offset:8192\n"
    "v_mfma_f32_16x16x32_bf16 v[48:51], v[96:99], v[116:119], v[48:51]\n"
    "ds_read_b64_tr_b16 v[146:147], v18 offset:9216\n"
    "v_mfma_f32_16x16x32_bf16 v[52:55], v[100:103], v[116:119], v[52:55]\n"
    "ds_read_b128 v[132:135], v16 offset:2048\n"
    "v_mfma_f32_16x16x32_bf16 v[22:25], v[160:163], v[112:115], v[22:25]\n"
    "ds_read_b64_tr_b16 v[148:149], v19 offset:8192\n"
    "v_mfma_f32_16x16x32_bf16 v[26:29], v[160:163], v[116:119], v[26:29]\n"
    "ds_read_b64_tr_b16 v[150:151], v19 offset:9216\n"
    "s_waitcnt lgkmcnt(9)\n"
    "v_mfma_f32_16x16x32_bf16 v[40:43], v[104:107], v[112:115], v[40:43]\n"
    "ds_read_b128 v[136:139], v17 offset:0\n"
    "v_mfma_f32_16x16x32_bf16 v[56:59], v[104:107], v[116:119], v[56:59]\n"
    "ds_read_b64_tr_b16 v[152:153], v20 offset:8192\n"
    "v_mfma_f32_16x16x32_bf16 v[64:67], v[96:99], v[120:123], v[64:67]\n"
    "ds_read_b64_tr_b16 v[154:155], v20 offset:9216\n"
    "v_mfma_f32_16x16x32_bf16 v[68:71], v[100:103], v[120:123], v[68:71]\n"
    "ds_read_b128 v[140:143], v17 offset:2048\n"
    "v_mfma_f32_16x16x32_bf16 v[72:75], v[104:107], v[120:123], v[72:75]\n"
    "ds_read_b64_tr_b16 v[156:157], v21 offset:8192\n"
    "s_waitcnt lgkmcnt(11)\n"
    "v_mfma_f32_16x16x32_bf16 v[44:47], v[108:111], v[112:115], v[44:47]\n"
    "ds_read_b64_tr_b16 v[158:159], v21 offset:9216\n"
    "v_mfma_f32_16x16x32_bf16 v[60:63], v[108:111], v[116:119], v[60:63]\n"
    "v_add_u32 v14, s26, v14\n"
    "v_add_u32 v15, s26, v15\n"
    "v_mfma_f32_16x16x32_bf16 v[76:79], v[108:111], v[120:123], v[76:79]\n"
    "v_add_u32 v16, s26, v16\n"
    "v_add_u32 v17, s26, v17\n"
    "v_mfma_f32_16x16x32_bf16 v[80:83], v[96:99], v[124:127], v[80:83]\n"
    "v_add_u32 v18, s27, v18\n"
    "v_add_u32 v19, s27, v19\n"
    "v_mfma_f32_16x16x32_bf16 v[84:87], v[100:103], v[124:127], v[84:87]\n"
    "v_add_u32 v20, s27, v20\n"
    "v_add_u32 v21, s27, v21\n"
    "v_mfma_f32_16x16x32_bf16 v[88:91], v[104:107], v[124:127], v[88:91]\n"
    "s_cmp_lg_u32 s24, 0\n"
    "v_mfma_f32_16x16x32_bf16 v[92:95], v[108:111], v[124:127], v[92:95]\n"
    "s_cbranch_scc1 1b\n"
    "s_waitcnt lgkmcnt(0)\n"
    "v_mfma_f32_16x16x32_bf16 v[22:25], v[160:163], v[144:147], v[22:25]\n"
    "v_mfma_f32_16x16x32_bf16 v[26:29], v[160:163], v[148:151], v[26:29]\n"
    "v_mfma_f32_16x16x32_bf16 v[32:35], v[128:131], v[144:147], v[32:35]\n"
    "v_mfma_f32_16x16x32_bf16 v[36:39], v[132:135], v[144:147], v[36:39]\n"
    "v_mfma_f32_16x16x32_bf16 v[48:51], v[128:131], v[148:151], v[48:51]\n"
    "s_mov_b64 exec, 0xffff\n"
    "s_nop 3\n"
    "ds_write_b32 v30, v22\n"
    "ds_write_b32 v30, v26 offset:64\n"
    "s_mov_b64 exec, -1\n"
    "v_mfma_f32_16x16x32_bf16 v[52:55], v[132:135], v[148:151], v[52:55]\n"
    "v_mfma_f32_16x16x32_bf16 v[40:43], v[136:139], v[144:147], v[40:43]\n"
    "v_mfma_f32_16x16x32_bf16 v[56:59], v[136:139], v[148:151], v[56:59]\n"
    "s_waitcnt lgkmcnt(0)\n"
    "s_barrier\n"
    "v_mfma_f32_16x16x32_bf16 v[64:67], v[128:131], v[152:155], v[64:67]\n"
    "ds_read_b128 v[96:99], v8 offset:0\n"
    "ds_read_b128 v[100:103], v8 offset:64\n"
    "v_mfma_f32_16x16x32_bf16 v[68:71], v[132:135], v[152:155], v[68:71]\n"
    "ds_read_b128 v[104:107], v8 offset:512\n"
    "ds_read_b128 v[108:111], v8 offset:576\n"
    "v_mfma_f32_16x16x32_bf16 v[72:75], v[136:139], v[152:155], v[72:75]\n"
    "ds_read2_b32 v[22:23], v10 offset1:16\n"
    "ds_read2_b32 v[24:25], v11 offset1:16\n"
    "v_mfma_f32_16x16x32_bf16 v[44:47], v[140:143], v[144:147], v[44:47]\n"
    "ds_read_b128 v[26:29], v12\n"
    "ds_read_b128 v[112:115], v9 offset:0\n"
    "v_mfma_f32_16x16x32_bf16 v[60:63], v[140:143], v[148:151], v[60:63]\n"
    "ds_read_b128 v[116:119], v9 offset:64\n"
    "ds_read_b128 v[120:123], v9 offset:512\n"
    "v_mfma_f32_16x16x32_bf16 v[76:79], v[140:143], v[152:155], v[76:79]\n"
    "ds_read_b128 v[124:127], v9 offset:576\n"
    "v_mfma_f32_16x16x32_bf16 v[80:83], v[128:131], v[156:159], v[80:83]\n"
    "v_mfma_f32_16x16x32_bf16 v[84:87], v[132:135], v[156:159], v[84:87]\n"
    "v_mfma_f32_16x16x32_bf16 v[88:91], v[136:139], v[156:159], v[88:91]\n"
    "v_mfma_f32_16x16x32_bf16 v[92:95], v[140:143], v[156:159], v[92:95]\n"
    "s_waitcnt lgkmcnt(0)\n"
    "s_setprio 3\n"
    "v_add_u32 v14, s31, v26\n"
    "v_add_u32 v15, s31, v27\n"
    "v_add_u32 v16, s31, v28\n"
    "v_add_u32 v17, s31, v29\n"
    "s_nop 3\n"
    "v_pk_fma_f32 v[32:33], v[22:23], v[96:97], v[32:33] op_sel:[0,0,0] op_sel_hi:[0,1,1]\n"
    "v_pk_fma_f32 v[34:35], v[22:23], v[98:99], v[34:35] op_sel:[0,0,0] op_sel_hi:[0,1,1]\n"
    "v_pk_fma_f32 v[36:37], v[22:23], v[100:101], v[36:37] op_sel:[0,0,0] op_sel_hi:[0,1,1]\n"
    "v_pk_fma_f32 v[38:39], v[22:23], v[102:103], v[38:39] op_sel:[0,0,0] op_sel_hi:[0,1,1]\n"
    "v_pk_mul_f32 v[32:33], v[104:105], v[32:33] op_sel:[0,0] op_sel_hi:[1,1]\n"
    "v_pk_mul_f32 v[34:35], v[106:107], v[34:35] op_sel:[0,0] op_sel_hi:[1,1]\n"
    "v_pk_mul_f32 v[36:37], v[108:109], v[36:37] op_sel:[0,0] op_sel_hi:[1,1]\n"
    "v_pk_mul_f32 v[38:39], v[110:111], v[38:39] op_sel:[0,0] op_sel_hi:[1,1]\n"
    "v_cvt_pk_bf16_f32 v128, v32, v36\n"
    "v_cvt_pk_bf16_f32 v129, v33, v37\n"
    "v_cvt_pk_bf16_f32 v130, v34, v38\n"
    "v_cvt_pk_bf16_f32 v131, v35, v39\n"
    "global_store_dwordx4 v26, v[128:131], s[40:41] sc1\n"
    "v_pk_fma_f32 v[48:49], v[22:23], v[96:97], v[48:49] op_sel:[1,0,0] op_sel_hi:[1,1,1]\n"
    "v_pk_fma_f32 v[50:51], v[22:23], v[98:99], v[50:51] op_sel:[1,0,0] op_sel_hi:[1,1,1]\n"
    "v_pk_fma_f32 v[52:53], v[22:23], v[100:101], v[52:53] op_sel:[1,0,0] op_sel_hi:[1,1,1]\n"
    "v_pk_fma_f32 v[54:55], v[22:23], v[102:103], v[54:55] op_sel:[1,0,0] op_sel_hi:[1,1,1]\n"
    "v_pk_mul_f32 v[48:49], v[104:105], v[48:49] op_sel:[0,0] op_sel_hi:[1,1]\n"
    "v_pk_mul_f32 v[50:51], v[106:107], v[50:51] op_sel:[0,0] op_sel_hi:[1,1]\n"
    "v_pk_mul_f32 v[52:53], v[108:109], v[52:53] op_sel:[0,0] op_sel_hi:[1,1]\n"
    "v_pk_mul_f32 v[54:55], v[110:111], v[54:55] op_sel:[0,0] op_sel_hi:[1,1]\n"
    "v_cvt_pk_bf16_f32 v132, v48, v52\n"
    "v_cvt_pk_bf16_f32 v133, v49, v53\n"
    "v_cvt_pk_bf16_f32 v134, v50, v54\n"
    "v_cvt_pk_bf16_f32 v135, v51, v55\n"
    "global_store_dwordx4 v27, v[132:135], s[40:41] sc1\n"
    "v_pk_fma_f32 v[64:65], v[24:25], v[96:97], v[64:65] op_sel:[0,0,0] op_sel_hi:[0,1,1]\n"
    "v_pk_fma_f32 v[66:67], v[24:25], v[98:99], v[66:67] op_sel:[0,0,0] op_sel_hi:[0,1,1]\n"
    "v_pk_fma_f32 v[68:69], v[24:25], v[100:101], v[68:69] op_sel:[0,0,0] op_sel_hi:[0,1,1]\n"
    "v_pk_fma_f32 v[70:71], v[24:25], v[102:103], v[70:71] op_sel:[0,0,0] op_sel_hi:[0,1,1]\n"
    "v_pk_mul_f32 v[64:65], v[104:105], v[64:65] op_sel:[0,0] op_sel_hi:[1,1]\n"
    "v_pk_mul_f32 v[66:67], v[106:107], v[66:67] op_sel:[0,0] op_sel_hi:[1,1]\n"
    "v_pk_mul_f32 v[68:69], v[108:109], v[68:69] op_sel:[0,0] op_sel_hi:[1,1]\n"
    "v_pk_mul_f32 v[70:71], v[110:111], v[70:71] op_sel:[0,0] op_sel_hi:[1,1]\n"
    "v_cvt_pk_bf16_f32 v136, v64, v68\n"
    "v_cvt_pk_bf16_f32 v137, v65, v69\n"
    "v_cvt_pk_bf16_f32 v138, v66, v70\n"
    "v_cvt_pk_bf16_f32 v139, v67, v71\n"
    "global_store_dwordx4 v28, v[136:139], s[40:41] sc1\n"
    "v_pk_fma_f32 v[80:81], v[24:25], v[96:97], v[80:81] op_sel:[1,0,0] op_sel_hi:[1,1,1]\n"
    "v_pk_fma_f32 v[82:83], v[24:25], v[98:99], v[82:83] op_sel:[1,0,0] op_sel_hi:[1,1,1]\n"
    "v_pk_fma_f32 v[84:85], v[24:25], v[100:101], v[84:85] op_sel:[1,0,0] op_sel_hi:[1,1,1]\n"
    "v_pk_fma_f32 v[86:87], v[24:25], v[102:103], v[86:87] op_sel:[1,0,0] op_sel_hi:[1,1,1]\n"
    "v_pk_mul_f32 v[80:81], v[104:105], v[80:81] op_sel:[0,0] op_sel_hi:[1,1]\n"
    "v_pk_mul_f32 v[82:83], v[106:107], v[82:83] op_sel:[0,0] op_sel_hi:[1,1]\n"
    "v_pk_mul_f32 v[84:85], v[108:109], v[84:85] op_sel:[0,0] op_sel_hi:[1,1]\n"
    "v_pk_mul_f32 v[86:87], v[110:111], v[86:87] op_sel:[0,0] op_sel_hi:[1,1]\n"
    "v_cvt_pk_bf16_f32 v140, v80, v84\n"
    "v_cvt_pk_bf16_f32 v141, v81, v85\n"
    "v_cvt_pk_bf16_f32 v142, v82, v86\n"
    "v_cvt_pk_bf16_f32 v143, v83, v87\n"
    "global_store_dwordx4 v29, v[140:143], s[40:41] sc1\n"
    "v_pk_fma_f32 v[40:41], v[22:23], v[112:113], v[40:41] op_sel:[0,0,0] op_sel_hi:[0,1,1]\n"
    "v_pk_fma_f32 v[42:43], v[22:23], v[114:115], v[42:43] op_sel:[0,0,0] op_sel_hi:[0,1,1]\n"
    "v_pk_fma_f32 v[44:45], v[22:23], v[116:117], v[44:45] op_sel:[0,0,0] op_sel_hi:[0,1,1]\n"
    "v_pk_fma_f32 v[46:47], v[22:23], v[118:119], v[46:47] op_sel:[0,0,0] op_sel_hi:[0,1,1]\n"
    "v_pk_mul_f32 v[40:41], v[120:121], v[40:41] op_sel:[0,0] op_sel_hi:[1,1]\n"
    "v_pk_mul_f32 v[42:43], v[122:123], v[42:43] op_sel:[0,0] op_sel_hi:[1,1]\n"
    "v_pk_mul_f32 v[44:45], v[124:125], v[44:45] op_sel:[0,0] op_sel_hi:[1,1]\n"
    "v_pk_mul_f32 v[46:47], v[126:127], v[46:47] op_sel:[0,0] op_sel_hi:[1,1]\n"
    "v_cvt_pk_bf16_f32 v144, v40, v44\n"
    "v_cvt_pk_bf16_f32 v145, v41, v45\n"
    "v_cvt_pk_bf16_f32 v146, v42, v46\n"
    "v_cvt_pk_bf16_f32 v147, v43, v47\n"
    "global_store_dwordx4 v14, v[144:147], s[40:41] sc1\n"
    "v_pk_fma_f32 v[56:57], v[22:23], v[112:113], v[56:57] op_sel:[1,0,0] op_sel_hi:[1,1,1]\n"
    "v_pk_fma_f32 v[58:59], v[22:23], v[114:115], v[58:59] op_sel:[1,0,0] op_sel_hi:[1,1,1]\n"
    "v_pk_fma_f32 v[60:61], v[22:23], v[116:117], v[60:61] op_sel:[1,0,0] op_sel_hi:[1,1,1]\n"
    "v_pk_fma_f32 v[62:63], v[22:23], v[118:119], v[62:63] op_sel:[1,0,0] op_sel_hi:[1,1,1]\n"
    "v_pk_mul_f32 v[56:57], v[120:121], v[56:57] op_sel:[0,0] op_sel_hi:[1,1]\n"
    "v_pk_mul_f32 v[58:59], v[122:123], v[58:59] op_sel:[0,0] op_sel_hi:[1,1]\n"
    "v_pk_mul_f32 v[60:61], v[124:125], v[60:61] op_sel:[0,0] op_sel_hi:[1,1]\n"
    "v_pk_mul_f32 v[62:63], v[126:127], v[62:63] op_sel:[0,0] op_sel_hi:[1,1]\n"
    "v_cvt_pk_bf16_f32 v148, v56, v60\n"
    "v_cvt_pk_bf16_f32 v149, v57, v61\n"
    "v_cvt_pk_bf16_f32 v150, v58, v62\n"
    "v_cvt_pk_bf16_f32 v151, v59, v63\n"
    "global_store_dwordx4 v15, v[148:151], s[40:41] sc1\n"
    "v_pk_fma_f32 v[72:73], v[24:25], v[112:113], v[72:73] op_sel:[0,0,0] op_sel_hi:[0,1,1]\n"
    "v_pk_fma_f32 v[74:75], v[24:25], v[114:115], v[74:75] op_sel:[0,0,0] op_sel_hi:[0,1,1]\n"
    "v_pk_fma_f32 v[76:77], v[24:25], v[116:117], v[76:77] op_sel:[0,0,0] op_sel_hi:[0,1,1]\n"
    "v_pk_fma_f32 v[78:79], v[24:25], v[118:119], v[78:79] op_sel:[0,0,0] op_sel_hi:[0,1,1]\n"
    "v_pk_mul_f32 v[72:73], v[120:121], v[72:73] op_sel:[0,0] op_sel_hi:[1,1]\n"
    "v_pk_mul_f32 v[74:75], v[122:123], v[74:75] op_sel:[0,0] op_sel_hi:[1,1]\n"
    "v_pk_mul_f32 v[76:77], v[124:125], v[76:77] op_sel:[0,0] op_sel_hi:[1,1]\n"
    "v_pk_mul_f32 v[78:79], v[126:127], v[78:79] op_sel:[0,0] op_sel_hi:[1,1]\n"
    "v_cvt_pk_bf16_f32 v152, v72, v76\n"
    "v_cvt_pk_bf16_f32 v153, v73, v77\n"
    "v_cvt_pk_bf16_f32 v154, v74, v78\n"
    "v_cvt_pk_bf16_f32 v155, v75, v79\n"
    "global_store_dwordx4 v16, v[152:155], s[40:41] sc1\n"
    "v_pk_fma_f32 v[88:89], v[24:25], v[112:113], v[88:89] op_sel:[1,0,0] op_sel_hi:[1,1,1]\n"
    "v_pk_fma_f32 v[90:91], v[24:25], v[114:115], v[90:91] op_sel:[1,0,0] op_sel_hi:[1,1,1]\n"
    "v_pk_fma_f32 v[92:93], v[24:25], v[116:117], v[92:93] op_sel:[1,0,0] op_sel_hi:[1,1,1]\n"
    "v_pk_fma_f32 v[94:95], v[24:25], v[118:119], v[94:95] op_sel:[1,0,0] op_sel_hi:[1,1,1]\n"
    "v_pk_mul_f32 v[88:89], v[120:121], v[88:89] op_sel:[0,0] op_sel_hi:[1,1]\n"
    "v_pk_mul_f32 v[90:91], v[122:123], v[90:91] op_sel:[0,0] op_sel_hi:[1,1]\n"
    "v_pk_mul_f32 v[92:93], v[124:125], v[92:93] op_sel:[0,0] op_sel_hi:[1,1]\n"
    "v_pk_mul_f32 v[94:95], v[126:127], v[94:95] op_sel:[0,0] op_sel_hi:[1,1]\n"
    "v_cvt_pk_bf16_f32 v156, v88, v92\n"
    "v_cvt_pk_bf16_f32 v157, v89, v93\n"
    "v_cvt_pk_bf16_f32 v158, v90, v94\n"
    "v_cvt_pk_bf16_f32 v159, v91, v95\n"
    "global_store_dwordx4 v17, v[156:159], s[40:41] sc1\n"
    "s_cmp_eq_u32 s34, 0\n"
    "s_cbranch_scc1 2f\n"
    "s_barrier\n"
    "2:\n"
    "s_nop 1\n"
    "9:\n"
    "v_mov_b32 v22, 0\n"
    "global_load_dwordx2 v[14:15], v22, s[40:41] sc1\n"
    "s_mul_i32 s23, s34, 8\n"
    "s_mov_b32 s27, 6\n"
    "v_mov_b32 v23, -1\n"
    "10:\n"
    "s_getreg_b32 s36, hwreg(HW_REG_IB_STS)\n"
    "s_and_b32 s36, s36, 0xc0000f\n"
    "s_cbranch_scc0 11f\n"
    "s_cmp_eq_u32 s23, 0\n"
    "s_cbranch_scc1 11f\n"
    "s_sub_u32 s27, s27, 1\n"
    "s_cbranch_scc0 10b\n"
    "s_barrier\n"
    "s_sub_u32 s23, s23, 1\n"
    "s_mov_b32 s27, 7\n"
    "s_branch 10b\n"
    "11:\n"
    "s_waitcnt vmcnt(0)\n"
    "v_readfirstlane_b32 s42, v14\n"
    "v_readfirstlane_b32 s43, v15\n"
    "s_add_u32 s42, s42, 1\n"
    "s_addc_u32 s43, s43, 0\n"
    "v_mov_b32 v14, s42\n"
    "v_mov_b32 v15, s43\n"
    "s_mov_b64 exec, 1\n"
    "global_store_dwordx2 v22, v[14:15], s[46:47] sc1\n"
    "s_mov_b64 exec, -1\n"
    "s_cmp_eq_u32 s23, 0\n"
    "s_cbranch_scc1 23f\n"
    "s_barrier\n"
    "s_sub_u32 s23, s23, 1\n"
    "s_mov_b32 s27, 7\n"
    "23:\n"
    "s_setprio 0\n"
    "v_mbcnt_lo_u32_b32 v140, -1, 0\n"
    "v_mbcnt_hi_u32_b32 v140, -1, v140\n"
    "v_lshrrev_b32 v141, 5, v140\n"
    "v_and_b32 v138, 31, v140\n"
    "v_mad_u32_u24 v135, v141, 20, v138\n"
    "v_lshlrev_b32 v135, 4, v135\n"
    "v_lshlrev_b32 v141, 9, v141\n"
    "v_lshl_add_u32 v134, v138, 3, v141\n"
    "v_and_b32 v139, 15, v140\n"
    "v_lshl_add_u32 v139, v139, 4, v141\n"
    "v_lshrrev_b32 v138, 4, v138\n"
    "v_lshl_add_u32 v139, v138, 3, v139\n"
    "s_mul_i32 s36, s19, 1280\n"
    "s_add_u32 s36, s36, 49152\n"
    "v_add_u32 v139, s36, v139\n"
    "v_mul_u32_u24 v108, 0x5000, v138\n"
    "v_and_b32 v109, 15, v140\n"
    "v_lshl_add_u32 v110, v109, 3, v108\n"
    "v_lshl_add_u32 v108, v109, 4, v108\n"
    "v_mul_u32_u24 v114, 320, v109\n"
    "v_lshrrev_b32 v109, 1, v141\n"
    "v_add_u32 v108, v108, v109\n"
    "v_lshlrev_b32 v109, 4, v140\n"
    "v_lshlrev_b32 v111, 3, v140\n"
    "s_lshr_b32 s18, s19, 2\n"
    "s_mul_i32 s36, s18, 51200\n"
    "v_add_u32 v112, s36, v109\n"
    "s_and_b32 s0, s19, 3\n"
    "s_mul_i32 s0, s0, 5120\n"
    "s_add_u32 s36, s36, s0\n"
    "v_xor_b32 v117, v111, v140\n"
    "v_and_b32 v117, 48, v117\n"
    "v_add3_u32 v114, v114, v117, s36\n"
    "s_lshl_b32 s36, s19, 6\n"
    "v_add_u32 v118, s36, v140\n"
    "v_mul_u32_u24 v117, 0xcccd, v118\n"
    "v_lshrrev_b32 v117, 19, v117\n"
    "v_mul_u32_u24 v116, 10, v117\n"
    "v_sub_u32 v118, v118, v116\n"
    "v_lshlrev_b32 v121, 4, v117\n"
    "v_lshl_add_u32 v119, v118, 4, s11\n"
    "v_mul_u32_u24 v115, 1280, v117\n"
    "s_mul_i32 s36, s10, 81920\n"
    "v_add3_u32 v115, v115, v119, s36\n"
    "v_mul_u32_u24 v116, 320, v117\n"
    "v_lshlrev_b32 v117, 3, v117\n"
    "v_and_b32 v117, 48, v117\n"
    "v_lshlrev_b32 v118, 5, v118\n"
    "v_xor_b32 v118, v118, v117\n"
    "v_add_u32 v116, v116, v118\n"
    "s_lshl_b32 s36, s10, 10\n"
    "s_add_u32 s36, s36, 237576\n"
    "v_add_u32 v118, s36, v121\n"
    "s_cmp_eq_u32 s23, 0\n"
    "s_cbranch_scc1 35f\n"
    "s_barrier\n"
    "s_sub_u32 s23, s23, 1\n"
    "s_mov_b32 s27, 7\n"
    "35:\n"
    "v_and_b32 v143, 7, v140\n"
    "v_lshlrev_b32 v143, 2, v143\n"
    "v_add_u32 v143, 61488, v143\n"
    "s_lshl_b32 s36, s19, 2\n"
    "s_add_u32 s36, s36, 61488\n"
    "v_mov_b32 v142, s36\n"
    "s_and_b32 s36, s19, 3\n"
    "s_lshl2_add_u32 s36, s10, s36\n"
    "s_mul_i32 s36, s36, 20480\n"
    "s_lshl_b32 s0, s18, 11\n"
    "s_lshl2_add_u32 s6, s0, s36\n"
    "s_add_u32 s0, s0, s36\n"
    "s_add_u32 s6, s6, 14684160\n"
    "s_add_u32 s0, s0, 14696448\n"
    "s_add_u32 s6, s40, s6\n"
    "s_addc_u32 s7, s41, 0\n"
    "s_add_u32 s0, s40, s0\n"
    "s_addc_u32 s1, s41, 0\n"
    "s_lshl3_add_u32 s36, s13, s19\n"
    "s_lshr_b32 s8, s36, 1\n"
    "s_mul_i32 s36, s36, 40960\n"
    "s_lshl_b32 s4, s12, 7\n"
    "s_lshl2_add_u32 s2, s4, s36\n"
    "s_lshl_b32 s8, s8, 12\n"
    "s_add_u32 s8, s8, s4\n"
    "s_add_u32 s4, s4, s36\n"
    "s_and_b32 s36, s19, 1\n"
    "s_lshl2_add_u32 s8, s36, s8\n"
    "s_add_u32 s2, s2, 14680064\n"
    "s_add_u32 s4, s4, 14696448\n"
    "s_add_u32 s8, s8, 270336\n"
    "s_add_u32 s2, s40, s2\n"
    "s_addc_u32 s3, s41, 0\n"
    "s_add_u32 s4, s40, s4\n"
    "s_addc_u32 s5, s41, 0\n"
    "s_add_u32 s8, s40, s8\n"
    "s_addc_u32 s9, s41, 0\n"
    "v_add_u32 v136, 133120, v134\n"
    "v_add_u32 v137, 133120, v135\n"
    "v_mov_b32 v138, 0xdddd\n"
    "v_mov_b32 v132, 0\n"
    "v_mov_b32 v133, 0\n"
    "v_mov_b32 v88, 0x3f803f80\n"
    "v_mov_b32 v89, 0\n"
    "v_mov_b32 v90, 0x3f803f80\n"
    "v_mov_b32 v91, 0\n"
    "v_mov_b32 v92, 0\n"
    "s_mov_b32 exec_lo, 0x11000000\n"
    "s_mov_b32 exec_hi, 0x11000000\n"
    "v_mov_b32 v92, 0x3f803f80\n"
    "s_mov_b64 exec, -1\n"
    "v_mov_b32 v93, v92\n"
    "v_mov_b32 v94, v92\n"
    "v_mov_b32 v95, v92\n"
    "v_mov_b32 v96, v92\n"
    "v_mov_b32 v97, v92\n"
    "v_mov_b32 v98, v92\n"
    "v_mov_b32 v99, v92\n"
    "v_mov_b32 v100, v92\n"
    "v_mov_b32 v101, v92\n"
    "v_mov_b32 v102, v92\n"
    "v_mov_b32 v103, v92\n"
    "v_mov_b32 v104, v92\n"
    "v_mov_b32 v105, v92\n"
    "v_mov_b32 v106, v92\n"
    "v_mov_b32 v107, v92\n"
    "v_mov_b32 v32, 0\n"
    "v_mov_b32 v33, 0\n"
    "v_mov_b32 v34, 0\n"
    "v_mov_b32 v35, 0\n"
    "v_mov_b32 v36, 0\n"
    "v_mov_b32 v37, 0\n"
    "v_mov_b32 v38, 0\n"
    "v_mov_b32 v39, 0\n"
    "v_mov_b32 v40, 0\n"
    "v_mov_b32 v41, 0\n"
    "v_mov_b32 v42, 0\n"
    "v_mov_b32 v43, 0\n"
    "v_mov_b32 v44, 0\n"
    "v_mov_b32 v45, 0\n"
    "v_mov_b32 v46, 0\n"
    "v_mov_b32 v47, 0\n"
    "v_mov_b32 v140, -1\n"
    "s_mov_b32 s27, 0\n"
    "s_cmp_lg_u32 s39, 0\n"
    "s_cbranch_scc1 31f\n"
    "s_cmp_lg_u32 s34, 0\n"
    "s_cbranch_scc1 24f\n"
    "s_cmp_eq_u32 s35, 15\n"
    "s_cselect_b32 s26, 0, 5\n"
    "32:\n"
    "s_sleep 4\n"
    "s_sub_u32 s26, s26, 1\n"
    "s_cbranch_scc0 32b\n"
    "24:\n"
    "s_cmp_le_u32 s23, 3\n"
    "s_cbranch_scc1 25f\n"
    "s_barrier\n"
    "s_sub_u32 s23, s23, 1\n"
    "s_mov_b32 s27, 7\n"
    "s_branch 24b\n"
    "25:\n"
    "s_mov_b32 s38, 0x80000\n"
    "26:\n"
    "global_load_dwordx4 v[48:51], v5, s[40:41] offset:0 sc1\n"
    "global_load_dwordx4 v[52:55], v5, s[40:41] offset:2560 sc1\n"
    "global_load_dwordx4 v[56:59], v6, s[40:41] offset:0 sc1\n"
    "12:\n"
    "s_getreg_b32 s36, hwreg(HW_REG_IB_STS)\n"
    "s_and_b32 s36, s36, 0xc0000f\n"
    "s_cbranch_scc0 13f\n"
    "s_cmp_eq_u32 s23, 0\n"
    "s_cbranch_scc1 13f\n"
    "s_sub_u32 s27, s27, 1\n"
    "s_cbranch_scc0 12b\n"
    "s_barrier\n"
    "s_sub_u32 s23, s23, 1\n"
    "s_mov_b32 s27, 7\n"
    "s_branch 12b\n"
    "13:\n"
    "s_waitcnt vmcnt(0)\n"
    "v_cmp_ne_u64 s[44:45], s[42:43], v[48:49]\n"
    "v_cmp_ne_u64 vcc, s[42:43], v[50:51]\n"
    "s_nop 0\n"
    "s_or_b64 s[44:45], vcc, s[44:45]\n"
    "v_cmp_ne_u64 s[20:21], s[42:43], v[52:53]\n"
    "v_cmp_ne_u64 vcc, s[42:43], v[54:55]\n"
    "s_nop 0\n"
    "s_or_b64 s[44:45], vcc, s[44:45]\n"
    "v_cmp_ne_u64 s[24:25], s[42:43], v[56:57]\n"
    "v_cmp_ne_u64 vcc, s[42:43], v[58:59]\n"
    "s_nop 0\n"
    "s_or_b64 s[44:45], vcc, s[44:45]\n"
    "s_or_b64 s[44:45], s[20:21], s[44:45]\n"
    "s_sub_u32 s27, s27, 2\n"
    "s_cselect_b32 s27, 0, s27\n"
    "s_or_b64 s[44:45], s[24:25], s[44:45]\n"
    "s_cbranch_scc0 27f\n"
    "s_cmp_lg_u32 s35, 15\n"
    "s_cbranch_scc1 36f\n"
    "s_sleep 10\n"
    "36:\n"
    "s_sub_u32 s38, s38, 1\n"
    "s_cbranch_scc0 26b\n"
    "27:\n"
    "s_mov_b64 s[44:45], s[48:49]\n"
    "s_mov_b32 m0, 49152\n"
    "s_nop 0\n"
    "global_load_lds_dwordx4 v4, s[44:45] offset:0 sc1\n"
    "global_load_lds_dwordx4 v4, s[44:45] offset:1024 sc1\n"
    "global_load_lds_dwordx4 v4, s[44:45] offset:2048 sc1\n"
    "global_load_lds_dwordx4 v4, s[44:45] offset:3072 sc1\n"
    "s_mov_b32 m0, 53248\n"
    "s_add_u32 s44, s44, 4096\n"
    "s_addc_u32 s45, s45, 0\n"
    "global_load_lds_dwordx4 v4, s[44:45] offset:0 sc1\n"
    "global_load_lds_dwordx4 v4, s[44:45] offset:1024 sc1\n"
    "global_load_lds_dwordx4 v4, s[44:45] offset:2048 sc1\n"
    "global_load_lds_dwordx4 v4, s[44:45] offset:3072 sc1\n"
    "s_mov_b32 m0, 57344\n"
    "s_add_u32 s44, s44, 4096\n"
    "s_addc_u32 s45, s45, 0\n"
    "global_load_lds_dwordx4 v4, s[44:45] offset:0 sc1\n"
    "global_load_lds_dwordx4 v4, s[44:45] offset:1024 sc1\n"
    "28:\n"
    "s_cmp_le_u32 s23, 7\n"
    "s_cbranch_scc1 29f\n"
    "s_barrier\n"
    "s_sub_u32 s23, s23, 1\n"
    "s_mov_b32 s27, 7\n"
    "s_branch 28b\n"
    "29:\n"
    "s_mov_b64 s[44:45], s[50:51]\n"
    "s_mov_b32 m0, 133120\n"
    "s_nop 0\n"
    "global_load_lds_dwordx4 v4, s[44:45] offset:0 sc1\n"
    "global_load_lds_dwordx4 v4, s[44:45] offset:1024 sc1\n"
    "global_load_lds_dwordx4 v4, s[44:45] offset:2048 sc1\n"
    "global_load_lds_dwordx4 v4, s[44:45] offset:3072 sc1\n"
    "s_mov_b32 m0, 137216\n"
    "s_add_u32 s44, s44, 4096\n"
    "s_addc_u32 s45, s45, 0\n"
    "global_load_lds_dwordx4 v4, s[44:45] offset:0 sc1\n"
    "global_load_lds_dwordx4 v4, s[44:45] offset:1024 sc1\n"
    "global_load_lds_dwordx4 v4, s[44:45] offset:2048 sc1\n"
    "global_load_lds_dwordx4 v4, s[44:45] offset:3072 sc1\n"
    "s_mov_b32 m0, 141312\n"
    "s_add_u32 s44, s44, 4096\n"
    "s_addc_u32 s45, s45, 0\n"
    "global_load_lds_dwordx4 v4, s[44:45] offset:0 sc1\n"
    "global_load_lds_dwordx4 v4, s[44:45] offset:1024 sc1\n"
    "global_load_lds_dwordx4 v4, s[44:45] offset:2048 sc1\n"
    "global_load_lds_dwordx4 v4, s[44:45] offset:3072 sc1\n"
    "s_mov_b32 m0, 145408\n"
    "s_add_u32 s44, s44, 4096\n"
    "s_addc_u32 s45, s45, 0\n"
    "global_load_lds_dwordx4 v4, s[44:45] offset:0 sc1\n"
    "global_load_lds_dwordx4 v4, s[44:45] offset:1024 sc1\n"
    "global_load_lds_dwordx4 v4, s[44:45] offset:2048 sc1\n"
    "global_load_lds_dwordx4 v4, s[44:45] offset:3072 sc1\n"
    "s_mov_b32 m0, 149504\n"
    "s_add_u32 s44, s44, 4096\n"
    "s_addc_u32 s45, s45, 0\n"
    "global_load_lds_dwordx4 v4, s[44:45] offset:0 sc1\n"
    "global_load_lds_dwordx4 v4, s[44:45] offset:1024 sc1\n"
    "global_load_lds_dwordx4 v4, s[44:45] offset:2048 sc1\n"
    "global_load_lds_dwordx4 v4, s[44:45] offset:3072 sc1\n"
    "s_mov_b32 m0, 153600\n"
    "s_add_u32 s44, s44, 4096\n"
    "s_addc_u32 s45, s45, 0\n"
    "global_load_lds_dwordx4 v4, s[44:45] offset:0 sc1\n"
    "global_load_lds_dwordx4 v4, s[44:45] offset:1024 sc1\n"
    "global_load_lds_dwordx4 v4, s[44:45] offset:2048 sc1\n"
    "global_load_lds_dwordx4 v4, s[44:45] offset:3072 sc1\n"
    "s_mov_b32 m0, 157696\n"
    "s_add_u32 s44, s44, 4096\n"
    "s_addc_u32 s45, s45, 0\n"
    "global_load_lds_dwordx4 v4, s[44:45] offset:0 sc1\n"
    "global_load_lds_dwordx4 v4, s[44:45] offset:1024 sc1\n"
    "global_load_lds_dwordx4 v4, s[44:45] offset:2048 sc1\n"
    "global_load_lds_dwordx4 v4, s[44:45] offset:3072 sc1\n"
    "s_mov_b32 m0, 161792\n"
    "s_add_u32 s44, s44, 4096\n"
    "s_addc_u32 s45, s45, 0\n"
    "global_load_lds_dwordx4 v4, s[44:45] offset:0 sc1\n"
    "global_load_lds_dwordx4 v4, s[44:45] offset:1024 sc1\n"
    "31:\n"
    "60:\n"
    "s_setprio 0\n"
    "5:\n"
    "s_cmp_eq_u32 s23, 0\n"
    "s_cbranch_scc1 6f\n"
    "s_barrier\n"
    "s_sub_u32 s23, s23, 1\n"
    "s_branch 5b\n"
    "6:\n"
    "s_mov_b32 s20, -1\n"
    "s_mov_b32 s21, 0\n"
    "s_mov_b32 s22, 0xfffff\n"
    "s_mov_b32 s23, 0xfffff\n"
    "s_mov_b32 s24, 0\n"
    "s_mov_b32 s28, 0x1016\n"
    "s_cmp_eq_u32 s18, 0\n"
    "s_cselect_b32 s28, 0x1202c, s28\n"
    "s_mov_b32 s25, 133120\n"
    "s_waitcnt vmcnt(25)\n"
    "ds_write_b32 v142, v140\n"
    "s_branch 7f\n"
    "4:\n"
    "s_sleep 1\n"
    "7:\n"
    "ds_read_b32 v141, v143\n"
    "s_waitcnt lgkmcnt(0)\n"
    "v_cmp_ne_u32 vcc, -1, v141\n"
    "s_cbranch_vccnz 4b\n"
    "s_setprio 0\n"
    "s_mov_b64 exec, s[20:21]\n"
    "ds_read_b64 v[132:133], v139 offset:1024\n"
    "ds_read_b64 v[88:89], v136 offset:1024\n"
    "ds_read_b64 v[90:91], v136 offset:3584\n"
    "s_mov_b64 exec, -1\n"
    "ds_read_b64 v[128:129], v139\n"
    "ds_read_b64 v[130:131], v139 offset:256\n"
    "ds_read_b64 v[80:81], v136 offset:0\n"
    "ds_read_b64 v[82:83], v136 offset:256\n"
    "ds_read_b64 v[84:85], v136 offset:2560\n"
    "ds_read_b64 v[86:87], v136 offset:2816\n"
    "s_mov_b64 exec, s[22:23]\n"
    "ds_read_b128 v[92:95], v137 offset:1280\n"
    "ds_read_b128 v[96:99], v137 offset:1920\n"
    "s_mov_b64 exec, -1\n"
    "s_waitcnt lgkmcnt(8)\n"
    "v_mfma_f32_32x32x8_bf16 v[48:63], v[88:89], v[132:133], 0\n"
    "v_mfma_f32_32x32x8_bf16 v[64:79], v[90:91], v[132:133], 0\n"
    "s_waitcnt lgkmcnt(4)\n"
    "v_mfma_f32_32x32x16_bf16 v[48:63], v[80:83], v[128:131], v[48:63]\n"
    "s_waitcnt lgkmcnt(2)\n"
    "v_mfma_f32_32x32x16_bf16 v[64:79], v[84:87], v[128:131], v[64:79]\n"
    "s_nop 9\n"
    "v_max_f32 v140, v48, v49\n"
    "v_max3_f32 v140, v50, v51, v140\n"
    "v_max3_f32 v140, v52, v53, v140\n"
    "v_max3_f32 v140, v54, v55, v140\n"
    "v_max3_f32 v140, v56, v57, v140\n"
    "v_max3_f32 v140, v58, v59, v140\n"
    "v_max3_f32 v140, v60, v61, v140\n"
    "v_max3_f32 v140, v62, v63, v140\n"
    "s_nop 2\n"
    "v_max3_f32 v140, v64, v65, v140\n"
    "v_max3_f32 v140, v66, v67, v140\n"
    "v_max3_f32 v140, v68, v69, v140\n"
    "v_max3_f32 v140, v70, v71, v140\n"
    "v_max3_f32 v140, v72, v73, v140\n"
    "v_max3_f32 v140, v74, v75, v140\n"
    "v_max3_f32 v140, v76, v77, v140\n"
    "v_max3_f32 v140, v78, v79, v140\n"
    "v_mov_b32 v141, v140\n"
    "s_nop 1\n"
    "v_permlane32_swap_b32 v140, v141\n"
    "s_nop 1\n"
    "v_max_f32 v140, v140, v141\n"
    "v_ceil_f32 v140, v140\n"
    "v_sub_f32 v140, 0xc2c80000, v140\n"
    "v_and_b32 v141, 0xffff0000, v140\n"
    "v_sub_f32 v141, v140, v141\n"
    "s_mov_b32 exec_lo, 0\n"
    "s_mov_b32 s27, 0x07060302\n"
    "v_perm_b32 v132, v141, v140, s27\n"
    "s_mov_b64 exec, -1\n"
    "v_and_b32 v141, 0xffff0000, v141\n"
    "v_and_b32 v140, 0xffff0000, v140\n"
    "v_add_f32 v140, v140, v141\n"
    "v_add_f32 v48, v140, v48\n"
    "v_add_f32 v49, v140, v49\n"
    "v_add_f32 v50, v140, v50\n"
    "v_add_f32 v51, v140, v51\n"
    "v_add_f32 v52, v140, v52\n"
    "v_add_f32 v53, v140, v53\n"
    "v_add_f32 v54, v140, v54\n"
    "v_add_f32 v55, v140, v55\n"
    "v_add_f32 v56, v140, v56\n"
    "v_add_f32 v57, v140, v57\n"
    "v_add_f32 v58, v140, v58\n"
    "v_add_f32 v59, v140, v59\n"
    "v_add_f32 v60, v140, v60\n"
    "v_add_f32 v61, v140, v61\n"
    "v_add_f32 v62, v140, v62\n"
    "v_add_f32 v63, v140, v63\n"
    "s_waitcnt vmcnt(0)\n"
    "global_load_dwordx4 v[120:123], v115, s[16:17]\n"
    "global_load_dwordx4 v[124:127], v119, s[14:15]\n"
    "global_load_dword v118, v118, s[40:41] sc1\n"
    "s_barrier\n"
    "s_nop 0\n"
    "s_cmp_lg_u32 s18, 0\n"
    "s_cbranch_scc1 2f\n"
    "1:\n"
    "s_setprio 0\n"
    "v_exp_f32 v48, v48\n"
    "s_setprio 1\n"
    "v_exp_f32 v49, v49\n"
    "v_exp_f32 v50, v50\n"
    "v_exp_f32 v51, v51\n"
    "v_exp_f32 v52, v52\n"
    "v_exp_f32 v53, v53\n"
    "v_exp_f32 v54, v54\n"
    "v_exp_f32 v55, v55\n"
    "v_exp_f32 v56, v56\n"
    "v_exp_f32 v57, v57\n"
    "v_exp_f32 v58, v58\n"
    "v_exp_f32 v59, v59\n"
    "v_exp_f32 v60, v60\n"
    "v_exp_f32 v61, v61\n"
    "v_exp_f32 v62, v62\n"
    "v_exp_f32 v63, v63\n"
    "s_setprio 2\n"
    "s_waitcnt lgkmcnt(0)\n"
    "v_mfma_f32_32x32x8_bf16 v[64:79], v[90:91], v[132:133], 0\n"
    "ds_read_b64 v[80:81], v136 offset:5120\n"
    "ds_read_b64 v[82:83], v136 offset:5376\n"
    "s_add_u32 s25, s25, 10240\n"
    "s_cmp_eq_u32 s25, 163840\n"
    "s_cselect_b32 s25, 0, s25\n"
    "v_mfma_f32_32x32x16_bf16 v[64:79], v[84:87], v[128:131], v[64:79]\n"
    "s_mov_b64 exec, s[20:21]\n"
    "ds_read_b64 v[88:89], v136 offset:6144\n"
    "s_mov_b64 exec, s[22:23]\n"
    "ds_read_b128 v[100:103], v137 offset:3840\n"
    "ds_read_b128 v[104:107], v137 offset:4480\n"
    "s_mov_b64 exec, -1\n"
    "v_smfmac_f32_32x32x32_bf16 v[32:47], v[92:95], v[48:55], v138\n"
    "s_nop 2\n"
    "v_smfmac_f32_32x32x32_bf16 v[32:47], v[96:99], v[56:63], v138\n"
    "s_setprio 0\n"
    "v_exp_f32 v64, v64\n"
    "s_setprio 1\n"
    "v_exp_f32 v65, v65\n"
    "v_exp_f32 v66, v66\n"
    "v_exp_f32 v67, v67\n"
    "v_exp_f32 v68, v68\n"
    "v_exp_f32 v69, v69\n"
    "v_exp_f32 v70, v70\n"
    "v_exp_f32 v71, v71\n"
    "v_exp_f32 v72, v72\n"
    "v_exp_f32 v73, v73\n"
    "v_exp_f32 v74, v74\n"
    "v_exp_f32 v75, v75\n"
    "v_exp_f32 v76, v76\n"
    "v_exp_f32 v77, v77\n"
    "v_exp_f32 v78, v78\n"
    "v_exp_f32 v79, v79\n"
    "s_setprio 2\n"
    "s_waitcnt lgkmcnt(0)\n"
    "v_mfma_f32_32x32x8_bf16 v[48:63], v[88:89], v[132:133], 0\n"
    "ds_read_b64 v[84:85], v136 offset:7680\n"
    "ds_read_b64 v[86:87], v136 offset:7936\n"
    "v_mfma_f32_32x32x16_bf16 v[48:63], v[80:83], v[128:131], v[48:63]\n"
    "s_mov_b64 exec, s[20:21]\n"
    "ds_read_b64 v[90:91], v136 offset:8704\n"
    "s_mov_b64 exec, s[22:23]\n"
    "ds_read_b128 v[92:95], v137 offset:6400\n"
    "ds_read_b128 v[96:99], v137 offset:7040\n"
    "s_mov_b64 exec, -1\n"
    "v_smfmac_f32_32x32x32_bf16 v[32:47], v[100:103], v[64:71], v138\n"
    "v_add_u32 v136, s25, v134\n"
    "s_nop 1\n"
    "v_smfmac_f32_32x32x32_bf16 v[32:47], v[104:107], v[72:79], v138\n"
    "s_setprio 0\n"
    "v_exp_f32 v48, v48\n"
    "s_setprio 1\n"
    "v_exp_f32 v49, v49\n"
    "v_exp_f32 v50, v50\n"
    "v_exp_f32 v51, v51\n"
    "v_exp_f32 v52, v52\n"
    "v_exp_f32 v53, v53\n"
    "v_exp_f32 v54, v54\n"
    "v_exp_f32 v55, v55\n"
    "v_exp_f32 v56, v56\n"
    "v_exp_f32 v57, v57\n"
    "v_exp_f32 v58, v58\n"
    "v_exp_f32 v59, v59\n"
    "v_exp_f32 v60, v60\n"
    "v_exp_f32 v61, v61\n"
    "v_exp_f32 v62, v62\n"
    "v_exp_f32 v63, v63\n"
    "s_setprio 2\n"
    "s_waitcnt lgkmcnt(0)\n"
    "v_mfma_f32_32x32x8_bf16 v[64:79], v[90:91], v[132:133], 0\n"
    "ds_read_b64 v[80:81], v136 offset:0\n"
    "ds_read_b64 v[82:83], v136 offset:256\n"
    "v_mfma_f32_32x32x16_bf16 v[64:79], v[84:87], v[128:131], v[64:79]\n"
    "s_mov_b64 exec, s[20:21]\n"
    "ds_read_b64 v[88:89], v136 offset:1024\n"
    "s_mov_b64 exec, s[22:23]\n"
    "ds_read_b128 v[100:103], v137 offset:8960\n"
    "ds_read_b128 v[104:107], v137 offset:9600\n"
    "s_mov_b64 exec, -1\n"
    "v_smfmac_f32_32x32x32_bf16 v[32:47], v[92:95], v[48:55], v138\n"
    "v_add_u32 v137, s25, v135\n"
    "s_bitcmp1_b32 s28, s24\n"
    "s_nop 0\n"
    "v_smfmac_f32_32x32x32_bf16 v[32:47], v[96:99], v[56:63], v138\n"
    "s_setprio 0\n"
    "v_exp_f32 v64, v64\n"
    "s_setprio 1\n"
    "v_exp_f32 v65, v65\n"
    "v_exp_f32 v66, v66\n"
    "v_exp_f32 v67, v67\n"
    "v_exp_f32 v68, v68\n"
    "v_exp_f32 v69, v69\n"
    "v_exp_f32 v70, v70\n"
    "v_exp_f32 v71, v71\n"
    "v_exp_f32 v72, v72\n"
    "v_exp_f32 v73, v73\n"
    "v_exp_f32 v74, v74\n"
    "v_exp_f32 v75, v75\n"
    "v_exp_f32 v76, v76\n"
    "v_exp_f32 v77, v77\n"
    "v_exp_f32 v78, v78\n"
    "v_exp_f32 v79, v79\n"
    "s_setprio 2\n"
    "s_waitcnt lgkmcnt(0)\n"
    "v_mfma_f32_32x32x8_bf16 v[48:63], v[88:89], v[132:133], 0\n"
    "ds_read_b64 v[84:85], v136 offset:2560\n"
    "ds_read_b64 v[86:87], v136 offset:2816\n"
    "v_mfma_f32_32x32x16_bf16 v[48:63], v[80:83], v[128:131], v[48:63]\n"
    "s_mov_b64 exec, s[20:21]\n"
    "ds_read_b64 v[90:91], v136 offset:3584\n"
    "s_mov_b64 exec, s[22:23]\n"
    "ds_read_b128 v[92:95], v137 offset:1280\n"
    "ds_read_b128 v[96:99], v137 offset:1920\n"
    "s_mov_b64 exec, -1\n"
    "v_smfmac_f32_32x32x32_bf16 v[32:47], v[100:103], v[64:71], v138\n"
    "s_add_u32 s24, s24, 1\n"
    "s_bitcmp0_b32 s28, s24\n"
    "s_nop 0\n"
    "v_smfmac_f32_32x32x32_bf16 v[32:47], v[104:107], v[72:79], v138\n"
    "s_cbranch_scc1 1b\n"
    "s_cmp_eq_u32 s24, 16\n"
    "s_cbranch_scc1 3f\n"
    "s_barrier\n"
    "s_branch 1b\n"
    "2:\n"
    "s_setprio 0\n"
    "v_exp_f32 v48, v48\n"
    "s_setprio 1\n"
    "v_exp_f32 v49, v49\n"
    "v_exp_f32 v50, v50\n"
    "v_exp_f32 v51, v51\n"
    "v_exp_f32 v52, v52\n"
    "v_exp_f32 v53, v53\n"
    "v_exp_f32 v54, v54\n"
    "v_exp_f32 v55, v55\n"
    "v_exp_f32 v56, v56\n"
    "v_exp_f32 v57, v57\n"
    "v_exp_f32 v58, v58\n"
    "v_exp_f32 v59, v59\n"
    "v_exp_f32 v60, v60\n"
    "v_exp_f32 v61, v61\n"
    "v_exp_f32 v62, v62\n"
    "v_exp_f32 v63, v63\n"
    "s_setprio 2\n"
    "s_waitcnt lgkmcnt(0)\n"
    "v_mfma_f32_32x32x8_bf16 v[64:79], v[90:91], v[132:133], 0\n"
    "ds_read_b64 v[80:81], v136 offset:5120\n"
    "ds_read_b64 v[82:83], v136 offset:5376\n"
    "s_add_u32 s25, s25, 10240\n"
    "s_cmp_eq_u32 s25, 163840\n"
    "s_cselect_b32 s25, 0, s25\n"
    "v_mfma_f32_32x32x16_bf16 v[64:79], v[84:87], v[128:131], v[64:79]\n"
    "s_mov_b64 exec, s[20:21]\n"
    "ds_read_b64 v[88:89], v136 offset:6144\n"
    "s_mov_b64 exec, s[22:23]\n"
    "ds_read_b128 v[100:103], v137 offset:3840\n"
    "ds_read_b128 v[104:107], v137 offset:4480\n"
    "s_mov_b64 exec, -1\n"
    "v_smfmac_f32_32x32x32_bf16 v[32:47], v[92:95], v[48:55], v138\n"
    "s_nop 2\n"
    "v_smfmac_f32_32x32x32_bf16 v[32:47], v[96:99], v[56:63], v138\n"
    "s_setprio 0\n"
    "v_exp_f32 v64, v64\n"
    "s_setprio 1\n"
    "v_exp_f32 v65, v65\n"
    "v_exp_f32 v66, v66\n"
    "v_exp_f32 v67, v67\n"
    "v_exp_f32 v68, v68\n"
    "v_exp_f32 v69, v69\n"
    "v_exp_f32 v70, v70\n"
    "v_exp_f32 v71, v71\n"
    "v_exp_f32 v72, v72\n"
    "v_exp_f32 v73, v73\n"
    "v_exp_f32 v74, v74\n"
    "v_exp_f32 v75, v75\n"
    "v_exp_f32 v76, v76\n"
    "v_exp_f32 v77, v77\n"
    "v_exp_f32 v78, v78\n"
    "v_exp_f32 v79, v79\n"
    "s_setprio 2\n"
    "s_waitcnt lgkmcnt(0)\n"
    "v_mfma_f32_32x32x8_bf16 v[48:63], v[88:89], v[132:133], 0\n"
    "ds_read_b64 v[84:85], v136 offset:7680\n"
    "ds_read_b64 v[86:87], v136 offset:7936\n"
    "v_mfma_f32_32x32x16_bf16 v[48:63], v[80:83], v[128:131], v[48:63]\n"
    "s_mov_b64 exec, s[20:21]\n"
    "ds_read_b64 v[90:91], v136 offset:8704\n"
    "s_mov_b64 exec, s[22:23]\n"
    "ds_read_b128 v[92:95], v137 offset:6400\n"
    "ds_read_b128 v[96:99], v137 offset:7040\n"
    "s_mov_b64 exec, -1\n"
    "v_smfmac_f32_32x32x32_bf16 v[32:47], v[100:103], v[64:71], v138\n"
    "v_add_u32 v136, s25, v134\n"
    "s_nop 1\n"
    "v_smfmac_f32_32x32x32_bf16 v[32:47], v[104:107], v[72:79], v138\n"
    "s_setprio 0\n"
    "v_exp_f32 v48, v48\n"
    "s_setprio 1\n"
    "v_exp_f32 v49, v49\n"
    "v_exp_f32 v50, v50\n"
    "v_exp_f32 v51, v51\n"
    "v_exp_f32 v52, v52\n"
    "v_exp_f32 v53, v53\n"
    "v_exp_f32 v54, v54\n"
    "v_exp_f32 v55, v55\n"
    "v_exp_f32 v56, v56\n"
    "v_exp_f32 v57, v57\n"
    "v_exp_f32 v58, v58\n"
    "v_exp_f32 v59, v59\n"
    "v_exp_f32 v60, v60\n"
    "v_exp_f32 v61, v61\n"
    "v_exp_f32 v62, v62\n"
    "v_exp_f32 v63, v63\n"
    "s_setprio 2\n"
    "s_waitcnt lgkmcnt(0)\n"
    "v_mfma_f32_32x32x8_bf16 v[64:79], v[90:91], v[132:133], 0\n"
    "ds_read_b64 v[80:81], v136 offset:0\n"
    "ds_read_b64 v[82:83], v136 offset:256\n"
    "v_mfma_f32_32x32x16_bf16 v[64:79], v[84:87], v[128:131], v[64:79]\n"
    "s_mov_b64 exec, s[20:21]\n"
    "ds_read_b64 v[88:89], v136 offset:1024\n"
    "s_mov_b64 exec, s[22:23]\n"
    "ds_read_b128 v[100:103], v137 offset:8960\n"
    "ds_read_b128 v[104:107], v137 offset:9600\n"
    "s_mov_b64 exec, -1\n"
    "v_smfmac_f32_32x32x32_bf16 v[32:47], v[92:95], v[48:55], v138\n"
    "v_add_u32 v137, s25, v135\n"
    "s_bitcmp1_b32 s28, s24\n"
    "s_nop 0\n"
    "v_smfmac_f32_32x32x32_bf16 v[32:47], v[96:99], v[56:63], v138\n"
    "s_setprio 0\n"
    "v_exp_f32 v64, v64\n"
    "s_setprio 1\n"
    "v_exp_f32 v65, v65\n"
    "v_exp_f32 v66, v66\n"
    "v_exp_f32 v67, v67\n"
    "v_exp_f32 v68, v68\n"
    "v_exp_f32 v69, v69\n"
    "v_exp_f32 v70, v70\n"
    "v_exp_f32 v71, v71\n"
    "v_exp_f32 v72, v72\n"
    "v_exp_f32 v73, v73\n"
    "v_exp_f32 v74, v74\n"
    "v_exp_f32 v75, v75\n"
    "v_exp_f32 v76, v76\n"
    "v_exp_f32 v77, v77\n"
    "v_exp_f32 v78, v78\n"
    "v_exp_f32 v79, v79\n"
    "s_setprio 2\n"
    "s_waitcnt lgkmcnt(0)\n"
    "s_cbranch_scc1 12f\n"
    "22:\n"
    "v_mfma_f32_32x32x8_bf16 v[48:63], v[88:89], v[132:133], 0\n"
    "ds_read_b64 v[84:85], v136 offset:2560\n"
    "ds_read_b64 v[86:87], v136 offset:2816\n"
    "v_mfma_f32_32x32x16_bf16 v[48:63], v[80:83], v[128:131], v[48:63]\n"
    "s_mov_b64 exec, s[20:21]\n"
    "ds_read_b64 v[90:91], v136 offset:3584\n"
    "s_mov_b64 exec, s[22:23]\n"
    "ds_read_b128 v[92:95], v137 offset:1280\n"
    "ds_read_b128 v[96:99], v137 offset:1920\n"
    "s_mov_b64 exec, -1\n"
    "v_smfmac_f32_32x32x32_bf16 v[32:47], v[100:103], v[64:71], v138\n"
    "s_add_u32 s24, s24, 1\n"
    "s_cmp_lt_u32 s24, 16\n"
    "s_nop 0\n"
    "v_smfmac_f32_32x32x32_bf16 v[32:47], v[104:107], v[72:79], v138\n"
    "s_cbranch_scc1 2b\n"
    "s_branch 3f\n"
    "12:\n"
    "s_barrier\n"
    "s_branch 22b\n"
    "3:\n"
    "s_setprio 3\n"
    "s_waitcnt vmcnt(0) lgkmcnt(0)\n"
    "s_mov_b64 exec, 1\n"
    "global_store_dword v109, v109, s[8:9] sc1\n"
    "s_mov_b64 exec, -1\n"
    "s_nop 0\n"
    "s_nop 3\n"
    "v_mul_f32 v140, 0x3b800000, v44\n"
    "v_rcp_f32 v140, v140\n"
    "v_mov_b32 v141, 0x203\n"
    "v_mul_f32 v140, 0x3b800000, v140\n"
    "v_cmp_class_f32 s[22:23], v44, v141\n"
    "v_cmp_gt_f32 vcc, 0xc000000, v44\n"
    "s_nop 0\n"
    "s_or_b64 s[22:23], vcc, s[22:23]\n"
    "v_mul_f32 v32, v140, v32\n"
    "v_mul_f32 v33, v140, v33\n"
    "v_mul_f32 v34, v140, v34\n"
    "v_mul_f32 v35, v140, v35\n"
    "v_mul_f32 v36, v140, v36\n"
    "v_mul_f32 v37, v140, v37\n"
    "v_mul_f32 v38, v140, v38\n"
    "v_mul_f32 v39, v140, v39\n"
    "v_mul_f32 v40, v140, v40\n"
    "v_mul_f32 v41, v140, v41\n"
    "v_mul_f32 v42, v140, v42\n"
    "v_mul_f32 v43, v140, v43\n"
    "v_add_f32_e64 v48, |v32|, |v33|\n"
    "v_add_f32_e64 v49, |v34|, |v35|\n"
    "v_add_f32_e64 v50, |v36|, |v37|\n"
    "v_add_f32_e64 v51, |v38|, |v39|\n"
    "v_add_f32_e64 v52, |v40|, |v41|\n"
    "v_add_f32_e64 v53, |v42|, |v43|\n"
    "v_add_f32 v48, v48, v49\n"
    "v_add_f32 v50, v50, v51\n"
    "v_add_f32 v52, v52, v53\n"
    "v_add_f32 v48, v48, v50\n"
    "v_add_f32 v48, v48, v52\n"
    "v_cmp_class_f32 vcc, v48, v141\n"
    "s_nop 1\n"
    "s_or_b64 vcc, vcc, s[22:23]\n"
    "s_cselect_b32 s29, 1, 0\n"
      : "={v[32:47]}"(o), "={v[128:131]}"(q1), "={v[132:133]}"(q2), "={v[108:111]}"(ta), "={v[112:127]}"(tb), "+{s29}"(bad), "+{s25}"(xs), "+{s24}"(s24), "+{v[14:17]}"(vc), "+{v[18:21]}"(vd), "+{v[8:11]}"(vb),
        "={s[0:1]}"(t0), "={s[2:3]}"(t2), "={s[4:5]}"(t4), "={s[6:7]}"(t6), "={s[8:9]}"(t8), "+{s[46:47]}"(pa)
      : "{v[4:7]}"(va), "{v12}"(ve), "{v30}"(vf), "{s10}"(rt), "{s11}"(cf * 160), "{s12}"(head), "{s13}"(qb), "{s[14:15]}"(b2), "{s[16:17]}"(x), "{s19}"(wid),
        "{s30}"(s30), "{s31}"(s31), "{s34}"(rfl(has2 ? 1 : 0)), "{s35}"(c), "{s39}"(w4), "{s[40:41]}"(ws), "{s[48:49]}"(pb), "{s[50:51]}"(pc)
      : "v22","v23","v24","v25","v26","v27","v28","v29","v31","v48","v49","v50","v51","v52","v53","v54","v55","v56","v57","v58","v59","v60","v61","v62","v63","v64","v65","v66","v67","v68","v69","v70","v71","v72","v73","v74","v75","v76","v77","v78","v79","v80","v81","v82","v83","v84","v85","v86","v87","v88","v89","v90","v91","v92","v93","v94","v95","v96","v97","v98","v99","v100","v101","v102","v103","v104","v105","v106","v107","v134","v135","v136","v137","v138","v139","v140","v141","v142","v143","v144","v145","v146","v147","v148","v149","v150","v151","v152","v153","v154","v155","v156","v157","v158","v159","v160","v161","v162","v163","s18","s20","s21","s22","s23","s26","s27","s28","s36","s37","s38","s42","s43","s44","s45","scc","memory","vcc");
    {
      if (__builtin_expect(bad, 0)) {
        LANE_VARS
        const unsigned r = lane & 31, hh = lane >> 5;
        if (hh) q2 = u32x2{0, 0};
        const unsigned ka0 = hh * 512 + r * 8, va0 = (hh * 20 + r) * 16;
        float m = -INFINITY; o = f32x16{};
        const unsigned onev = (r == 24 || r == 28) ? ONE2 : 0u;
        struct Tile { u32x2 a, b, c; u32x4 v1, v2; };
        auto fetch = [&](unsigned t) { const unsigned kb = WS_KV + head * 163840u + (t & 63) * 2560; return Tile{
          __builtin_amdgcn_raw_buffer_load_b64(wsr, kb + ka0, 0, 16), __builtin_amdgcn_raw_buffer_load_b64(wsr, kb + ka0 + 256, 0, 16), __builtin_amdgcn_raw_buffer_load_b64(wsr, kb + 1024 + r * 8, 0, 16),
          __builtin_amdgcn_raw_buffer_load_b128(wsr, kb + 1280 + va0, 0, 16), __builtin_amdgcn_raw_buffer_load_b128(wsr, kb + 1920 + va0, 0, 16)}; };
        Tile n0 = fetch(0), n1 = fetch(1);
#pragma unroll 1
        for (unsigned t = 0; t < 64; ++t) {
          const u32x2 a = n0.a, bk = n0.b, k2 = n0.c; u32x4 v1 = n0.v1, v2 = n0.v2;
          if (r >= 20) { v1 = u32x4{onev, onev, onev, onev}; v2 = v1; }
          n0 = n1; n1 = fetch(t + 2);
          f32x16 s = mfma32k8(k2, q2, f32x16{}); s = mfma32(u32x4{a[0], a[1], bk[0], bk[1]}, q1, s);
          float mn = m;
#pragma unroll
          for (int i = 0; i < 16; ++i) mn = fmaxf(mn, s[i]);
          mn = max_h(mn); const float al = __builtin_amdgcn_exp2f(m - mn); m = mn; u32x8 pa, pb;
#pragma unroll
          for (int i = 0; i < 8; ++i) { pa[i] = __float_as_uint(__builtin_amdgcn_exp2f(s[i] - mn)); pb[i] = __float_as_uint(__builtin_amdgcn_exp2f(s[8 + i] - mn)); }
          o *= al;
          o = __builtin_amdgcn_smfmac_f32_32x32x32_bf16(__builtin_bit_cast(bf16x8, v1), __builtin_bit_cast(bf16x16, pa), o, 0xdddd, 0, 0);
          o = __builtin_amdgcn_smfmac_f32_32x32x32_bf16(__builtin_bit_cast(bf16x8, v2), __builtin_bit_cast(bf16x16, pb), o, 0xdddd, 0, 0); }
        o *= __builtin_amdgcn_rcpf(o[12]);
      } }
    asm volatile(
    "v_cvt_pk_bf16_f32 v16, v32, v33\n"
    "v_cvt_pk_bf16_f32 v17, v34, v35\n"
    "v_cvt_pk_bf16_f32 v18, v36, v37\n"
    "v_cvt_pk_bf16_f32 v19, v38, v39\n"
    "v_cvt_pk_bf16_f32 v20, v40, v41\n"
    "v_cvt_pk_bf16_f32 v21, v42, v43\n"
    "v_min_u32 v16, -2, v16\n"
    "v_min_u32 v17, -2, v17\n"
    "v_min_u32 v18, -2, v18\n"
    "v_min_u32 v19, -2, v19\n"
    "v_min_u32 v20, -2, v20\n"
    "v_min_u32 v21, -2, v21\n"
    "global_store_dwordx4 v108, v[16:19], s[2:3] sc1\n"
    "s_mov_b32 exec_hi, 0\n"
    "global_store_dwordx2 v110, v[20:21], s[4:5] sc1\n"
    "s_mov_b32 exec_hi, -1\n"
    "v_mov_b32 v8, v109\n"
    "v_mov_b32 v9, v111\n"
    "s_setprio 0\n"
    "v_mov_b32 v10, v112\n"
    "s_mov_b32 s24, 0x40000\n"
    "s_mov_b32 s25, 0\n"
    "s_barrier\n"
    "ds_read_b128 v[84:87], v10 offset:0\n"
    "ds_read_b128 v[88:91], v10 offset:1024\n"
    "ds_read_b128 v[92:95], v10 offset:2048\n"
    "ds_read_b128 v[96:99], v10 offset:3072\n"
    "ds_read_b128 v[100:103], v10 offset:4096\n"
    "ds_read_b128 v[144:147], v10 offset:15360\n"
    "ds_read_b128 v[148:151], v10 offset:16384\n"
    "ds_read_b128 v[152:155], v10 offset:17408\n"
    "ds_read_b128 v[156:159], v10 offset:18432\n"
    "ds_read_b128 v[160:163], v10 offset:19456\n"
    "v_mov_b32 v11, v114\n"
    "v_mov_b32 v12, v115\n"
    "v_mov_b32 v13, v116\n"
    "v_lshlrev_b32 v16, 16, v120\n"
    "v_lshlrev_b32 v14, 16, v124\n"
    "v_add_f32 v16, v16, v14\n"
    "v_and_b32 v17, 0xffff0000, v120\n"
    "v_and_b32 v14, 0xffff0000, v124\n"
    "v_add_f32 v17, v17, v14\n"
    "v_lshlrev_b32 v18, 16, v121\n"
    "v_lshlrev_b32 v14, 16, v125\n"
    "v_add_f32 v18, v18, v14\n"
    "v_and_b32 v19, 0xffff0000, v121\n"
    "v_and_b32 v14, 0xffff0000, v125\n"
    "v_add_f32 v19, v19, v14\n"
    "v_lshlrev_b32 v20, 16, v122\n"
    "v_lshlrev_b32 v14, 16, v126\n"
    "v_add_f32 v20, v20, v14\n"
    "v_and_b32 v21, 0xffff0000, v122\n"
    "v_and_b32 v14, 0xffff0000, v126\n"
    "v_add_f32 v21, v21, v14\n"
    "v_lshlrev_b32 v22, 16, v123\n"
    "v_lshlrev_b32 v14, 16, v127\n"
    "v_add_f32 v22, v22, v14\n"
    "v_and_b32 v23, 0xffff0000, v123\n"
    "v_and_b32 v14, 0xffff0000, v127\n"
    "v_add_f32 v23, v23, v14\n"
    "v_mul_f32 v5, v118, v118\n"
    "ds_read_b128 v[104:107], v10 offset:5120\n"
    "ds_read_b128 v[108:111], v10 offset:6144\n"
    "ds_read_b128 v[112:115], v10 offset:7168\n"
    "ds_read_b128 v[116:119], v10 offset:8192\n"
    "ds_read_b128 v[120:123], v10 offset:9216\n"
    "s_waitcnt lgkmcnt(10)\n"
    "ds_read_b128 v[124:127], v10 offset:10240\n"
    "ds_read_b128 v[128:131], v10 offset:11264\n"
    "ds_read_b128 v[132:135], v10 offset:12288\n"
    "ds_read_b128 v[136:139], v10 offset:13312\n"
    "ds_read_b128 v[140:143], v10 offset:14336\n"
    "s_waitcnt lgkmcnt(0)\n"
    "s_barrier\n"
    "global_load_dwordx4 v[44:47], v8, s[6:7] offset:-4096 sc1\n"
    "global_load_dwordx4 v[48:51], v8, s[6:7] offset:-3072 sc1\n"
    "s_sleep 3\n"
    "global_load_dwordx4 v[52:55], v8, s[6:7] offset:-2048 sc1\n"
    "global_load_dwordx4 v[56:59], v8, s[6:7] offset:-1024 sc1\n"
    "s_sleep 3\n"
    "global_load_dwordx4 v[60:63], v8, s[6:7] offset:0 sc1\n"
    "global_load_dwordx4 v[64:67], v8, s[6:7] offset:1024 sc1\n"
    "s_waitcnt vmcnt(4)\n"
    "v_max3_u32 v14, v44, v45, v46\n"
    "v_max3_u32 v15, v47, v48, v49\n"
    "v_max3_u32 v14, v50, v51, v14\n"
    "v_max3_u32 v14, v14, v15, v15\n"
    "v_cmp_eq_u32 vcc, -1, v14\n"
    "s_mov_b32 s23, 0\n"
    "s_cbranch_vccnz 30f\n"
    "40:\n"
    "v_mfma_f32_16x16x32_bf16 v[24:27], v[84:87], v[44:47], 0\n"
    "global_load_dwordx4 v[68:71], v8, s[6:7] offset:2048 sc1\n"
    "v_mfma_f32_16x16x32_bf16 v[28:31], v[88:91], v[44:47], 0\n"
    "v_mfma_f32_16x16x32_bf16 v[32:35], v[92:95], v[44:47], 0\n"
    "global_load_dwordx4 v[72:75], v8, s[6:7] offset:3072 sc1\n"
    "v_mfma_f32_16x16x32_bf16 v[36:39], v[96:99], v[44:47], 0\n"
    "v_mfma_f32_16x16x32_bf16 v[40:43], v[100:103], v[44:47], 0\n"
    "v_mfma_f32_16x16x32_bf16 v[24:27], v[104:107], v[48:51], v[24:27]\n"
    "ds_read_b128 v[84:87], v10 offset:20480\n"
    "v_mfma_f32_16x16x32_bf16 v[28:31], v[108:111], v[48:51], v[28:31]\n"
    "ds_read_b128 v[88:91], v10 offset:21504\n"
    "v_mfma_f32_16x16x32_bf16 v[32:35], v[112:115], v[48:51], v[32:35]\n"
    "ds_read_b128 v[92:95], v10 offset:22528\n"
    "s_waitcnt vmcnt(4)\n"
    "v_max3_u32 v14, v52, v53, v54\n"
    "v_mfma_f32_16x16x32_bf16 v[36:39], v[116:119], v[48:51], v[36:39]\n"
    "ds_read_b128 v[96:99], v10 offset:23552\n"
    "v_max3_u32 v15, v55, v56, v57\n"
    "v_max3_u32 v14, v58, v59, v14\n"
    "v_mfma_f32_16x16x32_bf16 v[40:43], v[120:123], v[48:51], v[40:43]\n"
    "ds_read_b128 v[100:103], v10 offset:24576\n"
    "v_max3_u32 v14, v14, v15, v15\n"
    "v_cmp_eq_u32 vcc, -1, v14\n"
    "s_mov_b32 s23, 1\n"
    "s_cbranch_vccnz 30f\n"
    "41:\n"
    "v_mfma_f32_16x16x32_bf16 v[24:27], v[124:127], v[52:55], v[24:27]\n"
    "ds_read_b128 v[104:107], v10 offset:25600\n"
    "global_load_dwordx2 v[76:77], v9, s[0:1] offset:0 sc1\n"
    "global_load_dwordx2 v[78:79], v9, s[0:1] offset:512 sc1\n"
    "v_mfma_f32_16x16x32_bf16 v[28:31], v[128:131], v[52:55], v[28:31]\n"
    "ds_read_b128 v[108:111], v10 offset:26624\n"
    "v_mfma_f32_16x16x32_bf16 v[32:35], v[132:135], v[52:55], v[32:35]\n"
    "ds_read_b128 v[112:115], v10 offset:27648\n"
    "global_load_dwordx2 v[80:81], v9, s[0:1] offset:1024 sc1\n"
    "global_load_dwordx2 v[82:83], v9, s[0:1] offset:1536 sc1\n"
    "v_mfma_f32_16x16x32_bf16 v[36:39], v[136:139], v[52:55], v[36:39]\n"
    "ds_read_b128 v[116:119], v10 offset:28672\n"
    "v_mfma_f32_16x16x32_bf16 v[40:43], v[140:143], v[52:55], v[40:43]\n"
    "ds_read_b128 v[120:123], v10 offset:29696\n"
    "v_mfma_f32_16x16x32_bf16 v[24:27], v[144:147], v[56:59], v[24:27]\n"
    "ds_read_b128 v[124:127], v10 offset:30720\n"
    "v_mfma_f32_16x16x32_bf16 v[28:31], v[148:151], v[56:59], v[28:31]\n"
    "ds_read_b128 v[128:131], v10 offset:31744\n"
    "v_mfma_f32_16x16x32_bf16 v[32:35], v[152:155], v[56:59], v[32:35]\n"
    "ds_read_b128 v[132:135], v10 offset:32768\n"
    "s_waitcnt vmcnt(6)\n"
    "v_max3_u32 v14, v60, v61, v62\n"
    "v_mfma_f32_16x16x32_bf16 v[36:39], v[156:159], v[56:59], v[36:39]\n"
    "ds_read_b128 v[136:139], v10 offset:33792\n"
    "v_max3_u32 v15, v63, v64, v65\n"
    "v_max3_u32 v14, v66, v67, v14\n"
    "v_mfma_f32_16x16x32_bf16 v[40:43], v[160:163], v[56:59], v[40:43]\n"
    "ds_read_b128 v[140:143], v10 offset:34816\n"
    "v_max3_u32 v14, v14, v15, v15\n"
    "v_cmp_eq_u32 vcc, -1, v14\n"
    "s_mov_b32 s23, 2\n"
    "s_cbranch_vccnz 30f\n"
    "42:\n"
    "s_waitcnt lgkmcnt(10)\n"
    "v_mfma_f32_16x16x32_bf16 v[24:27], v[84:87], v[60:63], v[24:27]\n"
    "ds_read_b128 v[144:147], v10 offset:35840\n"
    "v_mfma_f32_16x16x32_bf16 v[28:31], v[88:91], v[60:63], v[28:31]\n"
    "ds_read_b128 v[148:151], v10 offset:36864\n"
    "v_mfma_f32_16x16x32_bf16 v[32:35], v[92:95], v[60:63], v[32:35]\n"
    "ds_read_b128 v[152:155], v10 offset:37888\n"
    "v_mfma_f32_16x16x32_bf16 v[36:39], v[96:99], v[60:63], v[36:39]\n"
    "ds_read_b128 v[156:159], v10 offset:38912\n"
    "v_mfma_f32_16x16x32_bf16 v[40:43], v[100:103], v[60:63], v[40:43]\n"
    "ds_read_b128 v[160:163], v10 offset:39936\n"
    "s_waitcnt lgkmcnt(10)\n"
    "v_mfma_f32_16x16x32_bf16 v[24:27], v[104:107], v[64:67], v[24:27]\n"
    "ds_read_b128 v[84:87], v10 offset:40960\n"
    "v_mfma_f32_16x16x32_bf16 v[28:31], v[108:111], v[64:67], v[28:31]\n"
    "ds_read_b128 v[88:91], v10 offset:41984\n"
    "v_mfma_f32_16x16x32_bf16 v[32:35], v[112:115], v[64:67], v[32:35]\n"
    "ds_read_b128 v[92:95], v10 offset:43008\n"
    "s_waitcnt vmcnt(4)\n"
    "v_max3_u32 v14, v68, v69, v70\n"
    "v_mfma_f32_16x16x32_bf16 v[36:39], v[116:119], v[64:67], v[36:39]\n"
    "ds_read_b128 v[96:99], v10 offset:44032\n"
    "v_max3_u32 v15, v71, v72, v73\n"
    "v_max3_u32 v14, v74, v75, v14\n"
    "v_mfma_f32_16x16x32_bf16 v[40:43], v[120:123], v[64:67], v[40:43]\n"
    "ds_read_b128 v[100:103], v10 offset:45056\n"
    "v_max3_u32 v14, v14, v15, v15\n"
    "v_cmp_eq_u32 vcc, -1, v14\n"
    "s_mov_b32 s23, 3\n"
    "s_cbranch_vccnz 30f\n"
    "43:\n"
    "s_waitcnt lgkmcnt(10)\n"
    "v_mfma_f32_16x16x32_bf16 v[24:27], v[124:127], v[68:71], v[24:27]\n"
    "ds_read_b128 v[104:107], v10 offset:46080\n"
    "v_mfma_f32_16x16x32_bf16 v[28:31], v[128:131], v[68:71], v[28:31]\n"
    "ds_read_b128 v[108:111], v10 offset:47104\n"
    "v_mfma_f32_16x16x32_bf16 v[32:35], v[132:135], v[68:71], v[32:35]\n"
    "ds_read_b128 v[112:115], v10 offset:48128\n"
    "v_mfma_f32_16x16x32_bf16 v[36:39], v[136:139], v[68:71], v[36:39]\n"
    "ds_read_b128 v[116:119], v10 offset:49152\n"
    "v_mfma_f32_16x16x32_bf16 v[40:43], v[140:143], v[68:71], v[40:43]\n"
    "ds_read_b128 v[120:123], v10 offset:50176\n"
    "s_waitcnt lgkmcnt(10)\n"
    "v_mfma_f32_16x16x32_bf16 v[24:27], v[144:147], v[72:75], v[24:27]\n"
    "v_mfma_f32_16x16x32_bf16 v[28:31], v[148:151], v[72:75], v[28:31]\n"
    "v_mfma_f32_16x16x32_bf16 v[32:35], v[152:155], v[72:75], v[32:35]\n"
    "s_waitcnt vmcnt(0)\n"
    "v_max3_u32 v14, v76, v77, v78\n"
    "v_mfma_f32_16x16x32_bf16 v[36:39], v[156:159], v[72:75], v[36:39]\n"
    "v_max3_u32 v15, v79, v80, v81\n"
    "v_max3_u32 v14, v82, v83, v14\n"
    "v_mfma_f32_16x16x32_bf16 v[40:43], v[160:163], v[72:75], v[40:43]\n"
    "v_max3_u32 v14, v14, v15, v15\n"
    "v_cmp_eq_u32 vcc, -1, v14\n"
    "s_mov_b32 s23, 4\n"
    "s_cbranch_vccnz 30f\n"
    "44:\n"
    "s_waitcnt lgkmcnt(5)\n"
    "v_mfma_f32_16x16x32_bf16 v[24:27], v[84:87], v[76:79], v[24:27]\n"
    "v_mfma_f32_16x16x32_bf16 v[28:31], v[88:91], v[76:79], v[28:31]\n"
    "v_mfma_f32_16x16x32_bf16 v[32:35], v[92:95], v[76:79], v[32:35]\n"
    "v_mfma_f32_16x16x32_bf16 v[36:39], v[96:99], v[76:79], v[36:39]\n"
    "v_mfma_f32_16x16x32_bf16 v[40:43], v[100:103], v[76:79], v[40:43]\n"
    "s_waitcnt lgkmcnt(0)\n"
    "v_mfma_f32_16x16x32_bf16 v[24:27], v[104:107], v[80:83], v[24:27]\n"
    "v_mfma_f32_16x16x32_bf16 v[28:31], v[108:111], v[80:83], v[28:31]\n"
    "v_mfma_f32_16x16x32_bf16 v[32:35], v[112:115], v[80:83], v[32:35]\n"
    "v_mfma_f32_16x16x32_bf16 v[36:39], v[116:119], v[80:83], v[36:39]\n"
    "v_mfma_f32_16x16x32_bf16 v[40:43], v[120:123], v[80:83], v[40:43]\n"
    "s_nop 4\n"
    "ds_write_b128 v11, v[24:27] offset:0\n"
    "ds_write_b128 v11, v[28:31] offset:64\n"
    "ds_write_b128 v11, v[32:35] offset:128\n"
    "ds_write_b128 v11, v[36:39] offset:192\n"
    "ds_write_b128 v11, v[40:43] offset:256\n"
    "s_branch 50f\n"
    "30:\n"
    "s_waitcnt vmcnt(0)\n"
    "s_mov_b32 s25, 0\n"
    "v_max3_u32 v14, v44, v45, v46\n"
    "v_max3_u32 v15, v47, v48, v49\n"
    "v_max3_u32 v14, v50, v51, v14\n"
    "v_max_u32 v14, v14, v15\n"
    "v_cmp_eq_u32 vcc, -1, v14\n"
    "s_nop 1\n"
    "s_cmp_lg_u64 vcc, 0\n"
    "s_cselect_b32 s22, 1, 0\n"
    "s_or_b32 s25, s25, s22\n"
    "v_max3_u32 v14, v52, v53, v54\n"
    "v_max3_u32 v15, v55, v56, v57\n"
    "v_max3_u32 v14, v58, v59, v14\n"
    "v_max_u32 v14, v14, v15\n"
    "v_cmp_eq_u32 vcc, -1, v14\n"
    "s_nop 1\n"
    "s_cmp_lg_u64 vcc, 0\n"
    "s_cselect_b32 s22, 2, 0\n"
    "s_or_b32 s25, s25, s22\n"
    "v_max3_u32 v14, v60, v61, v62\n"
    "v_max3_u32 v15, v63, v64, v65\n"
    "v_max3_u32 v14, v66, v67, v14\n"
    "v_max_u32 v14, v14, v15\n"
    "v_cmp_eq_u32 vcc, -1, v14\n"
    "s_nop 1\n"
    "s_cmp_lg_u64 vcc, 0\n"
    "s_cselect_b32 s22, 4, 0\n"
    "s_or_b32 s25, s25, s22\n"
    "v_max3_u32 v14, v68, v69, v70\n"
    "v_max3_u32 v15, v71, v72, v73\n"
    "v_max3_u32 v14, v74, v75, v14\n"
    "v_max_u32 v14, v14, v15\n"
    "v_cmp_eq_u32 vcc, -1, v14\n"
    "s_nop 1\n"
    "s_cmp_lg_u64 vcc, 0\n"
    "s_cselect_b32 s22, 8, 0\n"
    "s_or_b32 s25, s25, s22\n"
    "v_max3_u32 v14, v76, v77, v78\n"
    "v_max3_u32 v15, v79, v80, v81\n"
    "v_max3_u32 v14, v82, v83, v14\n"
    "v_max_u32 v14, v14, v15\n"
    "v_cmp_eq_u32 vcc, -1, v14\n"
    "s_nop 1\n"
    "s_cmp_lg_u64 vcc, 0\n"
    "s_cselect_b32 s22, 16, 0\n"
    "s_or_b32 s25, s25, s22\n"
    "s_sub_u32 s24, s24, 1\n"
    "s_max_i32 s24, s24, 0\n"
    "s_cselect_b32 s25, s25, 0\n"
    "s_bitcmp0_b32 s25, s23\n"
    "s_cbranch_scc1 36f\n"
    "s_sleep 4\n"
    "s_bitcmp0_b32 s25, 0\n"
    "s_cbranch_scc1 31f\n"
    "global_load_dwordx4 v[44:47], v8, s[6:7] offset:-4096 sc1\n"
    "global_load_dwordx4 v[48:51], v8, s[6:7] offset:-3072 sc1\n"
    "31:\n"
    "s_bitcmp0_b32 s25, 1\n"
    "s_cbranch_scc1 32f\n"
    "global_load_dwordx4 v[52:55], v8, s[6:7] offset:-2048 sc1\n"
    "global_load_dwordx4 v[56:59], v8, s[6:7] offset:-1024 sc1\n"
    "32:\n"
    "s_bitcmp0_b32 s25, 2\n"
    "s_cbranch_scc1 33f\n"
    "global_load_dwordx4 v[60:63], v8, s[6:7] offset:0 sc1\n"
    "global_load_dwordx4 v[64:67], v8, s[6:7] offset:1024 sc1\n"
    "33:\n"
    "s_bitcmp0_b32 s25, 3\n"
    "s_cbranch_scc1 34f\n"
    "global_load_dwordx4 v[68:71], v8, s[6:7] offset:2048 sc1\n"
    "global_load_dwordx4 v[72:75], v8, s[6:7] offset:3072 sc1\n"
    "34:\n"
    "s_bitcmp0_b32 s25, 4\n"
    "s_cbranch_scc1 35f\n"
    "global_load_dwordx2 v[76:77], v9, s[0:1] offset:0 sc1\n"
    "global_load_dwordx2 v[78:79], v9, s[0:1] offset:512 sc1\n"
    "global_load_dwordx2 v[80:81], v9, s[0:1] offset:1024 sc1\n"
    "global_load_dwordx2 v[82:83], v9, s[0:1] offset:1536 sc1\n"
    "35:\n"
    "s_branch 30b\n"
    "36:\n"
    "s_cmp_eq_u32 s23, 0\n"
    "s_cbranch_scc1 40b\n"
    "s_cmp_eq_u32 s23, 1\n"
    "s_cbranch_scc1 41b\n"
    "s_cmp_eq_u32 s23, 2\n"
    "s_cbranch_scc1 42b\n"
    "s_cmp_eq_u32 s23, 3\n"
    "s_cbranch_scc1 43b\n"
    "s_branch 44b\n"
    "50:\n"
      : "={v[16:23]}"(xb), "={v12}"(xo), "={v13}"(a0), "={v5}"(r2), "+{v[32:47]}"(o), "+{v[108:111]}"(ta), "+{v[112:127]}"(tb) : "{s[0:1]}"(t0), "{s[2:3]}"(t2), "{s[4:5]}"(t4), "{s[6:7]}"(t6), "{s[8:9]}"(t8) : "v8","v9","v10","v11","v14","v15","v24","v25","v26","v27","v28","v29","v30","v31","v48","v49","v50","v51","v52","v53","v54","v55","v56","v57","v58","v59","v60","v61","v62","v63","v64","v65","v66","v67","v68","v69","v70","v71","v72","v73","v74","v75","v76","v77","v78","v79","v80","v81","v82","v83","v84","v85","v86","v87","v88","v89","v90","v91","v92","v93","v94","v95","v96","v97","v98","v99","v100","v101","v102","v103","v104","v105","v106","v107","v128","v129","v130","v131","v132","v133","v134","v135","v136","v137","v138","v139","v140","v141","v142","v143","v144","v145","v146","v147","v148","v149","v150","v151","v152","v153","v154","v155","v156","v157","v158","v159","v160","v161","v162","v163","s22","s23","s24","s25","scc","memory","vcc");
  } else {
  {
    u64 tag;
    __builtin_amdgcn_s_setprio(2);
    u32x16 o0, o1;
    if (w4 == 0) { LANE_VARS
      *(LAS u32x4*)(L_SYNC + lane * 16) = u32x4{0, 0, 0, 0};
      const unsigned pk = (lane - 12) * 8 + cf, ph = (lane - 20) & 31, pbi = (rt >> 2) * 4 + (ph & 3);
      *(LAS u32x4*)(L_SYNC + 1024 + lane * 16) = u32x4{
        WS_FLAGH + 8 * fidx(lane < 12 ? rt >> 1 : (lane < 20 ? pk >> 2 : ph & 15), lane < 12 ? lane + 2 * (lane / 6) : (lane < 20 ? 6 + (pk & 1) + 4 * (pk & 2) : ((ph >> 4) ^ (((pbi + 4) >> 5) & pbi)) * 8 + (pbi >> 2))),
        L_RED + l15 * 320 + ((g ^ ((l15 >> 1) & 3)) << 4), (lane >> 5) * 20480 + (lane & 31) * 16, (lane >> 3) * 20480 + (lane & 7) * 16}; }
    if (c != 15) {
      LANE_VARS
      const char* bp = b1 + ((has2 ? p2 : 0) * 128 + 64 * wn + 32 * wm) * 2;
      {
        const unsigned sec = c / 5, c5 = c - sec * 5, ts = sec == 0 ? 1280u : 2560u; u32x4 dst;
#pragma unroll
        for (int n = 0; n < 4; ++n) { const unsigned tn = n ^ (2 * wm);
          const unsigned fs = c5 * 128 + 64 * wn + 16 * tn + l15, hv = fs / 20, ch = c5 * 32 + 16 * wn + 4 * tn + g, hq = ch / 5;
          dst[n] = (sec == 2 ? WS_KV + hv * 163840u + 1280 + (g * 20 + fs - hv * 20) * 16 : (sec == 0 ? WS_Q + hq * 81920u : WS_KV + hq * 163840u) + (ch - hq * 5) * 256 + l15 * 16) + (4 * rb + 2 * wm + wn) * ts; }
        *(LAS u32x4*)(L_Q + w4 * 1024 + lane * 16) = dst; }
      GEMM_ADDR(4, 1)
      const unsigned er = L_SYNC + 2048 + (64 * wm + l15) * 16, ec = L_STAT + (64 * wn + l15) * 16;
      unsigned xs = 4, wsl = 1; u32x4 axv = ax;
      asm volatile(
    "s_mov_b32 s31, 6\n"
    "s_mov_b32 s46, 0xfffec000\n"
    "v_mov_b32 v160, 0x3f803f80\n"
    "v_mov_b32 v161, 0x3f803f80\n"
    "v_mov_b32 v162, 0x3f803f80\n"
    "v_mov_b32 v163, 0x3f803f80\n"
    "s_mov_b32 s24, 9\n"
    "s_mov_b32 s28, 16384\n"
    "s_add_u32 s25, s25, 1\n"
    "s_cmp_eq_u32 s25, s31\n"
    "s_cselect_b32 s25, 0, s25\n"
    "s_cselect_b32 s26, s46, s28\n"
    "s_cselect_b32 s31, 4, s31\n"
    "s_cselect_b32 s46, 0xffff4000, s46\n"
    "s_add_u32 s29, s29, 1\n"
    "s_cmp_eq_u32 s29, 3\n"
    "s_cselect_b32 s29, 0, s29\n"
    "s_cselect_b32 s27, 0xffff8000, s28\n"
    "v_mbcnt_lo_u32_b32 v31, -1, 0\n"
    "v_mbcnt_hi_u32_b32 v31, -1, v31\n"
    "v_lshlrev_b32 v166, 3, v31\n"
    "v_add_u32 v166, s30, v166\n"
    "v_and_b32 v31, 15, v31\n"
    "v_lshlrev_b32 v31, 1, v31\n"
    "v_mov_b32 v3, 0\n"
    "v_mov_b32 v7, 0\n"
    "s_barrier\n"
    "global_load_ushort v2, v31, s[44:45] offset:0\n"
    "global_load_ushort v6, v31, s[44:45] offset:32\n"
    "v_mov_b32 v26, 0\n"
    "global_load_dwordx2 v[24:25], v26, s[40:41] sc1\n"
    "s_barrier\n"
    "s_barrier\n"
    "s_waitcnt lgkmcnt(0)\n"
    "s_load_dword s35, s[48:49], 0\n"
    "s_load_dword s35, s[48:49], 128\n"
    "s_load_dword s35, s[48:49], 256\n"
    "s_load_dword s35, s[48:49], 3072\n"
    "s_load_dword s35, s[48:49], 3200\n"
    "s_load_dword s35, s[48:49], 3328\n"
    "s_load_dword s35, s[48:49], 6144\n"
    "s_load_dword s35, s[48:49], 6272\n"
    "s_load_dword s35, s[48:49], 6400\n"
    "s_load_dword s35, s[48:49], 9216\n"
    "s_load_dword s35, s[48:49], 9344\n"
    "s_load_dword s35, s[48:49], 9472\n"
    "s_load_dword s35, s[48:49], 12288\n"
    "s_load_dword s35, s[48:49], 12416\n"
    "s_load_dword s35, s[48:49], 12544\n"
    "s_barrier\n"
    "s_barrier\n"
    "s_barrier\n"
    "s_waitcnt lgkmcnt(0)\n"
    "s_load_dword s35, s[48:49], 64\n"
    "s_load_dword s35, s[48:49], 192\n"
    "s_load_dword s35, s[48:49], 320\n"
    "s_load_dword s35, s[48:49], 3136\n"
    "s_load_dword s35, s[48:49], 3264\n"
    "s_load_dword s35, s[48:49], 3392\n"
    "s_load_dword s35, s[48:49], 6208\n"
    "s_load_dword s35, s[48:49], 6336\n"
    "s_load_dword s35, s[48:49], 6464\n"
    "s_load_dword s35, s[48:49], 9280\n"
    "s_load_dword s35, s[48:49], 9408\n"
    "s_load_dword s35, s[48:49], 9536\n"
    "s_load_dword s35, s[48:49], 12352\n"
    "s_load_dword s35, s[48:49], 12480\n"
    "s_load_dword s35, s[48:49], 12608\n"
    "s_barrier\n"
    "s_barrier\n"
    "global_load_dwordx2 v[22:23], v167, s[40:41] sc1\n"
    "s_barrier\n"
    "s_barrier\n"
    "s_waitcnt vmcnt(1)\n"
    "v_readfirstlane_b32 s42, v24\n"
    "v_readfirstlane_b32 s43, v25\n"
    "s_add_u32 s42, s42, 1\n"
    "s_addc_u32 s43, s43, 0\n"
    "s_mov_b32 s36, 0x40000\n"
    "8:\n"
    "s_waitcnt vmcnt(0)\n"
    "v_cmp_ne_u32 vcc, s42, v23\n"
    "s_cbranch_vccz 9f\n"
    "global_load_dwordx2 v[22:23], v167, s[40:41] sc1\n"
    "s_sub_u32 s36, s36, 1\n"
    "s_cbranch_scc0 8b\n"
    "s_waitcnt vmcnt(0)\n"
    "9:\n"
    "ds_write_b32 v165, v22\n"
    "v_rcp_f32 v23, v22\n"
    "s_mov_b32 vcc_lo, 0xaaaaaaaa\n"
    "s_mov_b32 vcc_hi, 0xaaaaaaaa\n"
    "v_lshl_or_b32 v2, v2, 16, v2\n"
    "v_cndmask_b32 v23, v22, v23, vcc\n"
    "v_cvt_pk_bf16_f32 v24, v23, v23\n"
    "v_lshlrev_b32 v25, 16, v24\n"
    "v_sub_f32 v25, v23, v25\n"
    "v_cvt_pk_bf16_f32 v26, v23, v25\n"
    "v_cvt_pk_bf16_f32 v25, v25, v3\n"
    "v_cndmask_b32 v24, v24, v26, vcc\n"
    "v_cndmask_b32 v25, v25, v3, vcc\n"
    "v_lshl_or_b32 v6, v6, 16, v6\n"
    "ds_write_b64 v166, v[24:25]\n"
    "s_waitcnt lgkmcnt(0)\n"
    "s_barrier\n"
    "s_cmp_eq_u32 s34, 0\n"
    "s_cbranch_scc1 10f\n"
    "4:\n"
    "ds_read_b32 v31, v13\n"
    "s_waitcnt lgkmcnt(0)\n"
    "v_cmp_ne_u32 vcc, -1, v31\n"
    "s_cbranch_vccnz 4b\n"
    "ds_read_b128 v[96:99], v14 offset:0\n"
    "ds_read_b64_tr_b16 v[112:113], v18 offset:0\n"
    "ds_read_b64_tr_b16 v[114:115], v18 offset:1024\n"
    "ds_read_b128 v[100:103], v14 offset:2048\n"
    "ds_read_b64_tr_b16 v[116:117], v19 offset:0\n"
    "ds_read_b64_tr_b16 v[118:119], v19 offset:1024\n"
    "ds_read_b128 v[104:107], v15 offset:0\n"
    "ds_read_b64_tr_b16 v[120:121], v20 offset:0\n"
    "ds_read_b64_tr_b16 v[122:123], v20 offset:1024\n"
    "ds_read_b128 v[108:111], v15 offset:2048\n"
    "ds_read_b64_tr_b16 v[124:125], v21 offset:0\n"
    "ds_read_b64_tr_b16 v[126:127], v21 offset:1024\n"
    "s_waitcnt lgkmcnt(6)\n"
    "v_mfma_f32_16x16x32_bf16 v[32:35], v[112:115], v[96:99], 0\n"
    "ds_read_b128 v[128:131], v16 offset:0\n"
    "v_mfma_f32_16x16x32_bf16 v[36:39], v[112:115], v[100:103], 0\n"
    "ds_read_b64_tr_b16 v[144:145], v18 offset:8192\n"
    "v_mfma_f32_16x16x32_bf16 v[48:51], v[116:119], v[96:99], 0\n"
    "ds_read_b64_tr_b16 v[146:147], v18 offset:9216\n"
    "v_mfma_f32_16x16x32_bf16 v[52:55], v[116:119], v[100:103], 0\n"
    "ds_read_b128 v[132:135], v16 offset:2048\n"
    "v_mfma_f32_16x16x32_bf16 v[22:25], v[160:163], v[112:115], 0\n"
    "ds_read_b64_tr_b16 v[148:149], v19 offset:8192\n"
    "v_mfma_f32_16x16x32_bf16 v[26:29], v[160:163], v[116:119], 0\n"
    "ds_read_b64_tr_b16 v[150:151], v19 offset:9216\n"
    "s_waitcnt lgkmcnt(9)\n"
    "v_mfma_f32_16x16x32_bf16 v[40:43], v[112:115], v[104:107], 0\n"
    "ds_read_b128 v[136:139], v17 offset:0\n"
    "v_mfma_f32_16x16x32_bf16 v[56:59], v[116:119], v[104:107], 0\n"
    "ds_read_b64_tr_b16 v[152:153], v20 offset:8192\n"
    "v_mfma_f32_16x16x32_bf16 v[64:67], v[120:123], v[96:99], 0\n"
    "ds_read_b64_tr_b16 v[154:155], v20 offset:9216\n"
    "v_mfma_f32_16x16x32_bf16 v[68:71], v[120:123], v[100:103], 0\n"
    "ds_read_b128 v[140:143], v17 offset:2048\n"
    "v_mfma_f32_16x16x32_bf16 v[72:75], v[120:123], v[104:107], 0\n"
    "ds_read_b64_tr_b16 v[156:157], v21 offset:8192\n"
    "s_waitcnt lgkmcnt(11)\n"
    "v_mfma_f32_16x16x32_bf16 v[44:47], v[112:115], v[108:111], 0\n"
    "ds_read_b64_tr_b16 v[158:159], v21 offset:9216\n"
    "v_mfma_f32_16x16x32_bf16 v[60:63], v[116:119], v[108:111], 0\n"
    "v_add_u32 v14, s26, v14\n"
    "v_add_u32 v15, s26, v15\n"
    "v_mfma_f32_16x16x32_bf16 v[76:79], v[120:123], v[108:111], 0\n"
    "v_add_u32 v16, s26, v16\n"
    "v_add_u32 v17, s26, v17\n"
    "v_mfma_f32_16x16x32_bf16 v[80:83], v[124:127], v[96:99], 0\n"
    "v_add_u32 v18, s27, v18\n"
    "v_add_u32 v19, s27, v19\n"
    "v_mfma_f32_16x16x32_bf16 v[84:87], v[124:127], v[100:103], 0\n"
    "v_add_u32 v20, s27, v20\n"
    "v_add_u32 v21, s27, v21\n"
    "v_mfma_f32_16x16x32_bf16 v[88:91], v[124:127], v[104:107], 0\n"
    "v_mfma_f32_16x16x32_bf16 v[92:95], v[124:127], v[108:111], 0\n"
    "1:\n"
    "s_waitcnt lgkmcnt(0)\n"
    "s_barrier\n"
    "v_mfma_f32_16x16x32_bf16 v[32:35], v[144:147], v[128:131], v[32:35]\n"
    "ds_read_b128 v[96:99], v14 offset:0\n"
    "v_mfma_f32_16x16x32_bf16 v[36:39], v[144:147], v[132:135], v[36:39]\n"
    "ds_read_b64_tr_b16 v[112:113], v18 offset:0\n"
    "v_mfma_f32_16x16x32_bf16 v[48:51], v[148:151], v[128:131], v[48:51]\n"
    "ds_read_b64_tr_b16 v[114:115], v18 offset:1024\n"
    "v_mfma_f32_16x16x32_bf16 v[52:55], v[148:151], v[132:135], v[52:55]\n"
    "ds_read_b128 v[100:103], v14 offset:2048\n"
    "v_mfma_f32_16x16x32_bf16 v[22:25], v[160:163], v[144:147], v[22:25]\n"
    "ds_read_b64_tr_b16 v[116:117], v19 offset:0\n"
    "v_mfma_f32_16x16x32_bf16 v[26:29], v[160:163], v[148:151], v[26:29]\n"
    "ds_read_b64_tr_b16 v[118:119], v19 offset:1024\n"
    "v_mfma_f32_16x16x32_bf16 v[40:43], v[144:147], v[136:139], v[40:43]\n"
    "ds_read_b128 v[104:107], v15 offset:0\n"
    "v_mfma_f32_16x16x32_bf16 v[56:59], v[148:151], v[136:139], v[56:59]\n"
    "ds_read_b64_tr_b16 v[120:121], v20 offset:0\n"
    "v_mfma_f32_16x16x32_bf16 v[64:67], v[152:155], v[128:131], v[64:67]\n"
    "ds_read_b64_tr_b16 v[122:123], v20 offset:1024\n"
    "v_mfma_f32_16x16x32_bf16 v[68:71], v[152:155], v[132:135], v[68:71]\n"
    "ds_read_b128 v[108:111], v15 offset:2048\n"
    "v_mfma_f32_16x16x32_bf16 v[72:75], v[152:155], v[136:139], v[72:75]\n"
    "ds_read_b64_tr_b16 v[124:125], v21 offset:0\n"
    "v_mfma_f32_16x16x32_bf16 v[44:47], v[144:147], v[140:143], v[44:47]\n"
    "ds_read_b64_tr_b16 v[126:127], v21 offset:1024\n"
    "v_mfma_f32_16x16x32_bf16 v[60:63], v[148:151], v[140:143], v[60:63]\n"
    "s_add_u32 s25, s25, 1\n"
    "s_cmp_eq_u32 s25, s31\n"
    "v_mfma_f32_16x16x32_bf16 v[76:79], v[152:155], v[140:143], v[76:79]\n"
    "s_cselect_b32 s25, 0, s25\n"
    "s_cselect_b32 s26, s46, s28\n"
    "v_mfma_f32_16x16x32_bf16 v[80:83], v[156:159], v[128:131], v[80:83]\n"
    "s_cselect_b32 s31, 4, s31\n"
    "s_cselect_b32 s46, 0xffff4000, s46\n"
    "v_mfma_f32_16x16x32_bf16 v[84:87], v[156:159], v[132:135], v[84:87]\n"
    "s_add_u32 s29, s29, 1\n"
    "s_cmp_eq_u32 s29, 3\n"
    "v_mfma_f32_16x16x32_bf16 v[88:91], v[156:159], v[136:139], v[88:91]\n"
    "s_cselect_b32 s29, 0, s29\n"
    "s_cselect_b32 s27, 0xffff8000, s28\n"
    "v_mfma_f32_16x16x32_bf16 v[92:95], v[156:159], v[140:143], v[92:95]\n"
    "s_sub_u32 s24, s24, 1\n"
    "s_waitcnt lgkmcnt(6)\n"
    "v_mfma_f32_16x16x32_bf16 v[32:35], v[112:115], v[96:99], v[32:35]\n"
    "ds_read_b128 v[128:131], v16 offset:0\n"
    "v_mfma_f32_16x16x32_bf16 v[36:39], v[112:115], v[100:103], v[36:39]\n"
    "ds_read_b64_tr_b16 v[144:145], v18 offset:8192\n"
    "v_mfma_f32_16x16x32_bf16 v[48:51], v[116:119], v[96:99], v[48:51]\n"
    "ds_read_b64_tr_b16 v[146:147], v18 offset:9216\n"
    "v_mfma_f32_16x16x32_bf16 v[52:55], v[116:119], v[100:103], v[52:55]\n"
    "ds_read_b128 v[132:135], v16 offset:2048\n"
    "v_mfma_f32_16x16x32_bf16 v[22:25], v[160:163], v[112:115], v[22:25]\n"
    "ds_read_b64_tr_b16 v[148:149], v19 offset:8192\n"
    "v_mfma_f32_16x16x32_bf16 v[26:29], v[160:163], v[116:119], v[26:29]\n"
    "ds_read_b64_tr_b16 v[150:151], v19 offset:9216\n"
    "s_waitcnt lgkmcnt(9)\n"
    "v_mfma_f32_16x16x32_bf16 v[40:43], v[112:115], v[104:107], v[40:43]\n"
    "ds_read_b128 v[136:139], v17 offset:0\n"
    "v_mfma_f32_16x16x32_bf16 v[56:59], v[116:119], v[104:107], v[56:59]\n"
    "ds_read_b64_tr_b16 v[152:153], v20 offset:8192\n"
    "v_mfma_f32_16x16x32_bf16 v[64:67], v[120:123], v[96:99], v[64:67]\n"
    "ds_read_b64_tr_b16 v[154:155], v20 offset:9216\n"
    "v_mfma_f32_16x16x32_bf16 v[68:71], v[120:123], v[100:103], v[68:71]\n"
    "ds_read_b128 v[140:143], v17 offset:2048\n"
    "v_mfma_f32_16x16x32_bf16 v[72:75], v[120:123], v[104:107], v[72:75]\n"
    "ds_read_b64_tr_b16 v[156:157], v21 offset:8192\n"
    "s_waitcnt lgkmcnt(11)\n"
    "v_mfma_f32_16x16x32_bf16 v[44:47], v[112:115], v[108:111], v[44:47]\n"
    "ds_read_b64_tr_b16 v[158:159], v21 offset:9216\n"
    "v_mfma_f32_16x16x32_bf16 v[60:63], v[116:119], v[108:111], v[60:63]\n"
    "v_add_u32 v14, s26, v14\n"
    "v_add_u32 v15, s26, v15\n"
    "v_mfma_f32_16x16x32_bf16 v[76:79], v[120:123], v[108:111], v[76:79]\n"
    "v_add_u32 v16, s26, v16\n"
    "v_add_u32 v17, s26, v17\n"
    "v_mfma_f32_16x16x32_bf16 v[80:83], v[124:127], v[96:99], v[80:83]\n"
    "v_add_u32 v18, s27, v18\n"
    "v_add_u32 v19, s27, v19\n"
    "v_mfma_f32_16x16x32_bf16 v[84:87], v[124:127], v[100:103], v[84:87]\n"
    "v_add_u32 v20, s27, v20\n"
    "v_add_u32 v21, s27, v21\n"
    "v_mfma_f32_16x16x32_bf16 v[88:91], v[124:127], v[104:107], v[88:91]\n"
    "s_cmp_lg_u32 s24, 0\n"
    "v_mfma_f32_16x16x32_bf16 v[92:95], v[124:127], v[108:111], v[92:95]\n"
    "s_cbranch_scc1 1b\n"
    "s_waitcnt lgkmcnt(0)\n"
    "v_mfma_f32_16x16x32_bf16 v[22:25], v[160:163], v[144:147], v[22:25]\n"
    "v_mfma_f32_16x16x32_bf16 v[26:29], v[160:163], v[148:151], v[26:29]\n"
    "v_mfma_f32_16x16x32_bf16 v[32:35], v[144:147], v[128:131], v[32:35]\n"
    "ds_read_b128 v[96:99], v8 offset:0\n"
    "ds_read_b128 v[100:103], v8 offset:256\n"
    "v_mfma_f32_16x16x32_bf16 v[48:51], v[148:151], v[128:131], v[48:51]\n"
    "ds_read_b128 v[104:107], v9 offset:0\n"
    "ds_read_b128 v[108:111], v9 offset:256\n"
    "v_mfma_f32_16x16x32_bf16 v[36:39], v[144:147], v[132:135], v[36:39]\n"
    "s_nop 0\n"
    "s_nop 0\n"
    "v_mfma_f32_16x16x32_bf16 v[52:55], v[148:151], v[132:135], v[52:55]\n"
    "v_cvt_pk_bf16_f32 v1, v22, v3\n"
    "v_lshlrev_b32 v23, 16, v1\n"
    "v_mfma_f32_16x16x32_bf16 v[40:43], v[144:147], v[136:139], v[40:43]\n"
    "v_sub_f32 v23, v22, v23\n"
    "v_cvt_pk_bf16_f32 v0, v22, v23\n"
    "v_mfma_f32_16x16x32_bf16 v[56:59], v[148:151], v[136:139], v[56:59]\n"
    "v_cvt_pk_bf16_f32 v5, v26, v3\n"
    "v_lshlrev_b32 v27, 16, v5\n"
    "v_mfma_f32_16x16x32_bf16 v[44:47], v[144:147], v[140:143], v[44:47]\n"
    "v_sub_f32 v27, v26, v27\n"
    "v_cvt_pk_bf16_f32 v4, v26, v27\n"
    "v_mfma_f32_16x16x32_bf16 v[60:63], v[148:151], v[140:143], v[60:63]\n"
    "s_mov_b64 exec, 0xffff\n"
    "ds_write_b128 v30, v[0:3]\n"
    "ds_write_b128 v30, v[4:7] offset:256\n"
    "s_mov_b64 exec, -1\n"
    "ds_write_b32 v12, v31\n"
    "s_waitcnt lgkmcnt(3)\n"
    "v_mfma_f32_16x16x32_bf16 v[32:35], v[0:3], v[96:99], v[32:35]\n"
    "v_mfma_f32_16x16x32_bf16 v[48:51], v[4:7], v[96:99], v[48:51]\n"
    "v_mfma_f32_16x16x32_bf16 v[36:39], v[0:3], v[100:103], v[36:39]\n"
    "v_mfma_f32_16x16x32_bf16 v[52:55], v[4:7], v[100:103], v[52:55]\n"
    "v_mfma_f32_16x16x32_bf16 v[40:43], v[0:3], v[104:107], v[40:43]\n"
    "v_mfma_f32_16x16x32_bf16 v[56:59], v[4:7], v[104:107], v[56:59]\n"
    "v_mfma_f32_16x16x32_bf16 v[44:47], v[0:3], v[108:111], v[44:47]\n"
    "v_mfma_f32_16x16x32_bf16 v[60:63], v[4:7], v[108:111], v[60:63]\n"
    "v_mfma_f32_16x16x32_bf16 v[64:67], v[152:155], v[128:131], v[64:67]\n"
    "ds_read_b32 v164, v13 offset:16\n"
    "s_nop 0\n"
    "v_mfma_f32_16x16x32_bf16 v[80:83], v[156:159], v[128:131], v[80:83]\n"
    "v_mul_f32_e64 v32, v32, |v32|\n"
    "v_mul_f32_e64 v33, v33, |v33|\n"
    "v_mfma_f32_16x16x32_bf16 v[68:71], v[152:155], v[132:135], v[68:71]\n"
    "v_mul_f32_e64 v34, v34, |v34|\n"
    "v_mul_f32_e64 v35, v35, |v35|\n"
    "v_mfma_f32_16x16x32_bf16 v[84:87], v[156:159], v[132:135], v[84:87]\n"
    "v_mul_f32_e64 v48, v48, |v48|\n"
    "v_mul_f32_e64 v49, v49, |v49|\n"
    "3:\n"
    "s_waitcnt lgkmcnt(0)\n"
    "v_cmp_ne_u32 vcc, -1, v164\n"
    "s_cbranch_vccz 5f\n"
    "ds_read_b32 v164, v13 offset:16\n"
    "s_branch 3b\n"
    "5:\n"
    "ds_read_b128 v[0:3], v10\n"
    "ds_read_b128 v[4:7], v10 offset:256\n"
    "v_mfma_f32_16x16x32_bf16 v[72:75], v[152:155], v[136:139], v[72:75]\n"
    "v_mul_f32_e64 v50, v50, |v50|\n"
    "v_mul_f32_e64 v51, v51, |v51|\n"
    "v_cvt_pk_bf16_f32 v112, v32, v33\n"
    "v_mfma_f32_16x16x32_bf16 v[88:91], v[156:159], v[136:139], v[88:91]\n"
    "v_cvt_pk_bf16_f32 v113, v34, v35\n"
    "v_cvt_pk_bf16_f32 v114, v48, v49\n"
    "v_cvt_pk_bf16_f32 v115, v50, v51\n"
    "v_mfma_f32_16x16x32_bf16 v[76:79], v[152:155], v[140:143], v[76:79]\n"
    "v_pk_max_i16 v112, v112, 0\n"
    "v_pk_max_i16 v113, v113, 0\n"
    "v_pk_max_i16 v114, v114, 0\n"
    "v_mfma_f32_16x16x32_bf16 v[92:95], v[156:159], v[140:143], v[92:95]\n"
    "v_pk_max_i16 v115, v115, 0\n"
    "v_mul_f32_e64 v36, v36, |v36|\n"
    "v_mul_f32_e64 v37, v37, |v37|\n"
    "v_mul_f32_e64 v38, v38, |v38|\n"
    "v_mul_f32_e64 v39, v39, |v39|\n"
    "v_mul_f32_e64 v52, v52, |v52|\n"
    "v_mul_f32_e64 v53, v53, |v53|\n"
    "v_mul_f32_e64 v54, v54, |v54|\n"
    "v_mul_f32_e64 v55, v55, |v55|\n"
    "v_cvt_pk_bf16_f32 v116, v36, v37\n"
    "v_cvt_pk_bf16_f32 v117, v38, v39\n"
    "s_waitcnt lgkmcnt(0)\n"
    "v_mfma_f32_16x16x32_bf16 v[64:67], v[0:3], v[96:99], v[64:67]\n"
    "v_cvt_pk_bf16_f32 v118, v52, v53\n"
    "v_cvt_pk_bf16_f32 v119, v54, v55\n"
    "v_mfma_f32_16x16x32_bf16 v[80:83], v[4:7], v[96:99], v[80:83]\n"
    "v_pk_max_i16 v116, v116, 0\n"
    "v_pk_max_i16 v117, v117, 0\n"
    "v_mfma_f32_16x16x32_bf16 v[68:71], v[0:3], v[100:103], v[68:71]\n"
    "v_pk_max_i16 v118, v118, 0\n"
    "v_pk_max_i16 v119, v119, 0\n"
    "v_mfma_f32_16x16x32_bf16 v[84:87], v[4:7], v[100:103], v[84:87]\n"
    "v_mul_f32_e64 v40, v40, |v40|\n"
    "v_mul_f32_e64 v41, v41, |v41|\n"
    "v_mfma_f32_16x16x32_bf16 v[72:75], v[0:3], v[104:107], v[72:75]\n"
    "v_mul_f32_e64 v42, v42, |v42|\n"
    "v_mul_f32_e64 v43, v43, |v43|\n"
    "v_mfma_f32_16x16x32_bf16 v[88:91], v[4:7], v[104:107], v[88:91]\n"
    "v_mul_f32_e64 v56, v56, |v56|\n"
    "v_mul_f32_e64 v57, v57, |v57|\n"
    "v_mfma_f32_16x16x32_bf16 v[76:79], v[0:3], v[108:111], v[76:79]\n"
    "v_mul_f32_e64 v58, v58, |v58|\n"
    "v_mul_f32_e64 v59, v59, |v59|\n"
    "v_mfma_f32_16x16x32_bf16 v[92:95], v[4:7], v[108:111], v[92:95]\n"
    "v_cvt_pk_bf16_f32 v120, v40, v41\n"
    "v_cvt_pk_bf16_f32 v121, v42, v43\n"
    "v_cvt_pk_bf16_f32 v122, v56, v57\n"
    "v_cvt_pk_bf16_f32 v123, v58, v59\n"
    "v_pk_max_i16 v120, v120, 0\n"
    "v_pk_max_i16 v121, v121, 0\n"
    "v_pk_max_i16 v122, v122, 0\n"
    "v_pk_max_i16 v123, v123, 0\n"
    "v_mul_f32_e64 v44, v44, |v44|\n"
    "v_mul_f32_e64 v45, v45, |v45|\n"
    "v_mul_f32_e64 v46, v46, |v46|\n"
    "v_mul_f32_e64 v47, v47, |v47|\n"
    "v_mul_f32_e64 v60, v60, |v60|\n"
    "v_mul_f32_e64 v61, v61, |v61|\n"
    "v_mul_f32_e64 v62, v62, |v62|\n"
    "v_mul_f32_e64 v63, v63, |v63|\n"
    "v_cvt_pk_bf16_f32 v124, v44, v45\n"
    "v_cvt_pk_bf16_f32 v125, v46, v47\n"
    "v_cvt_pk_bf16_f32 v126, v60, v61\n"
    "v_cvt_pk_bf16_f32 v127, v62, v63\n"
    "v_pk_max_i16 v124, v124, 0\n"
    "v_pk_max_i16 v125, v125, 0\n"
    "v_pk_max_i16 v126, v126, 0\n"
    "v_pk_max_i16 v127, v127, 0\n"
    "v_mul_f32_e64 v64, v64, |v64|\n"
    "v_mul_f32_e64 v65, v65, |v65|\n"
    "v_mul_f32_e64 v66, v66, |v66|\n"
    "v_mul_f32_e64 v67, v67, |v67|\n"
    "v_mul_f32_e64 v80, v80, |v80|\n"
    "v_mul_f32_e64 v81, v81, |v81|\n"
    "v_mul_f32_e64 v82, v82, |v82|\n"
    "v_mul_f32_e64 v83, v83, |v83|\n"
    "v_cvt_pk_bf16_f32 v128, v64, v65\n"
    "v_cvt_pk_bf16_f32 v129, v66, v67\n"
    "v_cvt_pk_bf16_f32 v130, v80, v81\n"
    "v_cvt_pk_bf16_f32 v131, v82, v83\n"
    "v_pk_max_i16 v128, v128, 0\n"
    "v_pk_max_i16 v129, v129, 0\n"
    "v_pk_max_i16 v130, v130, 0\n"
    "v_pk_max_i16 v131, v131, 0\n"
    "v_mul_f32_e64 v68, v68, |v68|\n"
    "v_mul_f32_e64 v69, v69, |v69|\n"
    "v_mul_f32_e64 v70, v70, |v70|\n"
    "v_mul_f32_e64 v71, v71, |v71|\n"
    "v_mul_f32_e64 v84, v84, |v84|\n"
    "v_mul_f32_e64 v85, v85, |v85|\n"
    "v_mul_f32_e64 v86, v86, |v86|\n"
    "v_mul_f32_e64 v87, v87, |v87|\n"
    "v_cvt_pk_bf16_f32 v132, v68, v69\n"
    "v_cvt_pk_bf16_f32 v133, v70, v71\n"
    "v_cvt_pk_bf16_f32 v134, v84, v85\n"
    "v_cvt_pk_bf16_f32 v135, v86, v87\n"
    "v_pk_max_i16 v132, v132, 0\n"
    "v_pk_max_i16 v133, v133, 0\n"
    "v_pk_max_i16 v134, v134, 0\n"
    "v_pk_max_i16 v135, v135, 0\n"
    "v_mul_f32_e64 v72, v72, |v72|\n"
    "v_mul_f32_e64 v73, v73, |v73|\n"
    "v_mul_f32_e64 v74, v74, |v74|\n"
    "v_mul_f32_e64 v75, v75, |v75|\n"
    "v_mul_f32_e64 v88, v88, |v88|\n"
    "v_mul_f32_e64 v89, v89, |v89|\n"
    "v_mul_f32_e64 v90, v90, |v90|\n"
    "v_mul_f32_e64 v91, v91, |v91|\n"
    "v_cvt_pk_bf16_f32 v136, v72, v73\n"
    "v_cvt_pk_bf16_f32 v137, v74, v75\n"
    "v_cvt_pk_bf16_f32 v138, v88, v89\n"
    "v_cvt_pk_bf16_f32 v139, v90, v91\n"
    "v_pk_max_i16 v136, v136, 0\n"
    "v_pk_max_i16 v137, v137, 0\n"
    "v_pk_max_i16 v138, v138, 0\n"
    "v_pk_max_i16 v139, v139, 0\n"
    "v_mul_f32_e64 v76, v76, |v76|\n"
    "v_mul_f32_e64 v77, v77, |v77|\n"
    "v_mul_f32_e64 v78, v78, |v78|\n"
    "v_mul_f32_e64 v79, v79, |v79|\n"
    "v_mul_f32_e64 v92, v92, |v92|\n"
    "v_mul_f32_e64 v93, v93, |v93|\n"
    "v_mul_f32_e64 v94, v94, |v94|\n"
    "v_mul_f32_e64 v95, v95, |v95|\n"
    "v_cvt_pk_bf16_f32 v140, v76, v77\n"
    "v_cvt_pk_bf16_f32 v141, v78, v79\n"
    "v_cvt_pk_bf16_f32 v142, v92, v93\n"
    "v_cvt_pk_bf16_f32 v143, v94, v95\n"
    "v_pk_max_i16 v140, v140, 0\n"
    "v_pk_max_i16 v141, v141, 0\n"
    "v_pk_max_i16 v142, v142, 0\n"
    "v_pk_max_i16 v143, v143, 0\n"
    "10:\n"
    "s_barrier\n"
        : "={v[112:127]}"(o0), "={v[128:143]}"(o1), "+{s25}"(xs), "+{s29}"(wsl), "+{v[14:17]}"(axv), "+{v[18:21]}"(aw), "={s[42:43]}"(tag)
        : "{v30}"(ec + 512 * wm), "{v[8:11]}"(u32x4{g ? L_SYNC + 256 : er + 512 * wn, g ? L_SYNC + 256 : er + 512 * (1 - wn), ec + 512 * (1 - wm), 0}), "{s30}"(L_SYNC + 2048 + w4 * 512), "{s[48:49]}"(w1 + (((xcd & 3) * 32 + bi) * 5) * 3072 + (xcd >> 2) * 1536 + w4 * 384), "{s[44:45]}"(bp),
          "{v12}"(L_SYNC + 16 + w4 * 4), "{v13}"(L_SYNC + p4 * 4), "{v167}"(WS_ST + (rb * 128 + w4 * 32) * 16 + lane * 8), "{v165}"(L_STAT + (lane & 1) * 512 + w4 * 128 + (lane >> 1) * 4),
          "{s34}"(rfl(has2 ? 1 : 0)), "{s[40:41]}"(ws)
        : "v0","v1","v2","v3","v4","v5","v6","v7","v22","v23","v24","v25","v26","v27","v28","v29","v31","v32","v33","v34","v35","v36","v37","v38","v39","v40","v41","v42","v43","v44","v45","v46","v47","v48","v49","v50","v51","v52","v53","v54","v55","v56","v57","v58","v59","v60","v61","v62","v63","v64","v65","v66","v67","v68","v69","v70","v71","v72","v73","v74","v75","v76","v77","v78","v79","v80","v81","v82","v83","v84","v85","v86","v87","v88","v89","v90","v91","v92","v93","v94","v95","v96","v97","v98","v99","v100","v101","v102","v103","v104","v105","v106","v107","v108","v109","v110","v111","v144","v145","v146","v147","v148","v149","v150","v151","v152","v153","v154","v155","v156","v157","v158","v159","v160","v161","v162","v163","v164","v166","s24","s26","s27","s28","s31","s35","s36","s46","scc","memory", "vcc");
    } else { tag = read_tag(ws); asm volatile("s_waitcnt lgkmcnt(0)\n s_barrier\n s_barrier" : "={v[112:127]}"(o0), "={v[128:143]}"(o1) :: "memory"); }
    u32x4 vh; unsigned vf, ra, mk; const char* kvp = ws + WS_KV + head * 163840u + 30720;
    { const char* hs = ws + WS_H + ((rb * 8 + 4 * wm) * 48 + p2 * 4 + 2 * wn) * 1024; const char* ob = ws + WS_O + (qb * 16 + w4 * 4) * 20480u; const char* om = ob + head * 512;
      asm volatile(
    "v_mbcnt_lo_u32_b32 v8, -1, 0\n"
    "v_mbcnt_hi_u32_b32 v8, -1, v8\n"
    "v_lshlrev_b32 v8, 4, v8\n"
    "v_add_u32 v9, 62464, v8\n"
    "ds_read_b128 v[12:15], v9\n"
    "s_mul_i32 s34, s39, 5120\n"
    "s_add_u32 s26, s26, s34\n"
    "s_addc_u32 s27, s27, 0\n"
    "s_mov_b32 m0, s34\n"
    "s_nop 0\n"
    "global_load_lds_dwordx4 v8, s[26:27] offset:0 sc1\n"
    "global_load_lds_dwordx4 v8, s[26:27] offset:1024 sc1\n"
    "global_load_lds_dwordx4 v8, s[26:27] offset:2048 sc1\n"
    "global_load_lds_dwordx4 v8, s[26:27] offset:3072 sc1\n"
    "s_add_u32 m0, s34, 1024\n"
    "s_add_u32 s26, s26, 1024\n"
    "s_addc_u32 s27, s27, 0\n"
    "global_load_lds_dwordx4 v8, s[26:27] offset:3072 sc1\n"
    "s_add_u32 s34, s34, 20480\n"
    "s_add_u32 s26, s26, 19456\n"
    "s_addc_u32 s27, s27, 0\n"
    "s_mov_b32 m0, s34\n"
    "s_nop 0\n"
    "global_load_lds_dwordx4 v8, s[26:27] offset:0 sc1\n"
    "global_load_lds_dwordx4 v8, s[26:27] offset:1024 sc1\n"
    "global_load_lds_dwordx4 v8, s[26:27] offset:2048 sc1\n"
    "global_load_lds_dwordx4 v8, s[26:27] offset:3072 sc1\n"
    "s_add_u32 m0, s34, 1024\n"
    "s_add_u32 s26, s26, 1024\n"
    "s_addc_u32 s27, s27, 0\n"
    "global_load_lds_dwordx4 v8, s[26:27] offset:3072 sc1\n"
    "s_add_u32 s34, s34, 20480\n"
    "s_add_u32 s26, s26, 19456\n"
    "s_addc_u32 s27, s27, 0\n"
    "v_add_u32 v9, 49152, v8\n"
    "v_add_u32 v10, 98304, v8\n"
    "v_add_u32 v11, 147456, v8\n"
    "v_mov_b32 v16, -1\n"
    "v_mov_b32 v17, -1\n"
    "v_mov_b32 v18, -1\n"
    "v_mov_b32 v19, -1\n"
    "s_waitcnt lgkmcnt(0)\n"
    "s_cmp_eq_u32 s38, 0\n"
    "s_cbranch_scc1 16f\n"
    "global_store_dwordx4 v8, v[112:115], s[0:1] sc1\n"
    "global_store_dwordx4 v9, v[116:119], s[0:1] sc1\n"
    "global_store_dwordx4 v10, v[120:123], s[2:3] sc1\n"
    "global_store_dwordx4 v11, v[124:127], s[2:3] sc1\n"
    "global_store_dwordx4 v8, v[128:131], s[4:5] sc1\n"
    "global_store_dwordx4 v9, v[132:135], s[4:5] sc1\n"
    "global_store_dwordx4 v10, v[136:139], s[6:7] sc1\n"
    "global_store_dwordx4 v11, v[140:143], s[6:7] sc1\n"
    "16:\n"
    "global_store_dwordx4 v14, v[16:19], s[8:9] sc1\n"
    "s_add_u32 s8, s8, 40960\n"
    "s_addc_u32 s9, s9, 0\n"
    "global_store_dwordx4 v14, v[16:19], s[8:9] sc1\n"
    "s_mov_b32 exec_hi, 0\n"
    "global_store_dwordx4 v15, v[16:19], s[10:11] sc1\n"
    "s_mov_b64 exec, 1\n"
    "global_store_dwordx2 v8, v[16:17], s[12:13] sc1\n"
    "s_mov_b64 exec, -1\n"
        : "={v[8:11]}"(vh), "={v12}"(vf), "={v13}"(ra), "+{s[26:27]}"(kvp), "={s34}"(mk), "+{s[8:9]}"(om)
        : "{v[112:127]}"(o0), "{v[128:143]}"(o1), "{s38}"(rfl(has2 ? 1 : 0)), "{s39}"(w4), "{s[0:1]}"(hs + wn * 98304 + wm * 1024), "{s[2:3]}"(hs - wn * 98304 + wm * 1024),
          "{s[4:5]}"(hs + wn * 98304 + (1 - wm) * 1024), "{s[6:7]}"(hs - wn * 98304 + (1 - wm) * 1024), "{s[10:11]}"(ob + 16384 + head * 128), "{s[12:13]}"(ws + WS_HINT + (qb * 4 + w4) * 4096 + head * 128)
        : "v14","v15","v16","v17","v18","v19","s50","scc","memory"); }
    if (!has2) {
      LANE_VARS
#pragma unroll 1
      for (unsigned j = w4; j < (part < 6 ? 8u : 10u); j += 4) {
        const unsigned row = part < 6 ? 32 * j + 4 * g + q4 : (j < 8 ? 40 * j + 20 * (g >> 1) + 4 * (g & 1) + q4 : 160 * (j - 8) + 20 * g + 16 + q4);
        const unsigned a0 = L_X + row * 160 + p4 * 8, a1 = a0 + (part < 6 ? 2560 : (j < 8 ? 1280 : 12800));
        const unsigned d = WS_WP + pcf * 348160u + (part < 6 ? part * 8 + j : 48 + (part - 6) * 10 + j) * 5120 + lane * 16;
#pragma unroll
        for (int cc = 0; cc < 5; ++cc) { const u32x2 a = ldstr(a0 + cc * 32), bb = ldstr(a1 + cc * 32); __builtin_amdgcn_raw_buffer_store_b128(u32x4{a[0], a[1], bb[0], bb[1]}, wsr, d + cc * 1024, 0, 16); } }
    }
    {
      const char* wp = ws + WS_WP + cf * 348160u + w4 * 5120 + 2048; const char* wop = ws + WS_WP + cf * 348160u + 245760;
      const char* hp = ws + WS_H + rt * 196608 + w4 * 1024 + (1 - wn) * 98304; const char* hq = hp - (1 - wn) * 196608;
      const char* fh = ws + WS_FLAGH + fidx(rb, c) * 8;
      asm volatile(
    "s_setprio 2\n"
    "s_waitcnt vmcnt(12)\n"
    "s_barrier\n"
    "s_mov_b32 s24, 0\n"
    "4:\n"
    "s_mov_b32 m0, s34\n"
    "s_nop 0\n"
    "global_load_lds_dwordx4 v8, s[26:27] offset:0 sc1\n"
    "global_load_lds_dwordx4 v8, s[26:27] offset:1024 sc1\n"
    "global_load_lds_dwordx4 v8, s[26:27] offset:2048 sc1\n"
    "global_load_lds_dwordx4 v8, s[26:27] offset:3072 sc1\n"
    "s_add_u32 m0, s34, 1024\n"
    "s_add_u32 s26, s26, 1024\n"
    "s_addc_u32 s27, s27, 0\n"
    "global_load_lds_dwordx4 v8, s[26:27] offset:3072 sc1\n"
    "s_add_u32 s34, s34, 20480\n"
    "s_add_u32 s26, s26, 19456\n"
    "s_addc_u32 s27, s27, 0\n"
    "s_add_u32 s24, s24, 1\n"
    "s_cmp_lg_u32 s24, 2\n"
    "s_cbranch_scc1 5f\n"
    "s_waitcnt vmcnt(10)\n"
    "s_barrier\n"
    "s_cmp_lg_u32 s39, 0\n"
    "s_cbranch_scc1 5f\n"
    "v_mov_b32 v14, s42\n"
    "v_mov_b32 v15, s43\n"
    "s_mov_b64 exec, 1\n"
    "global_store_dwordx2 v8, v[14:15], s[44:45] sc1\n"
    "s_mov_b64 exec, -1\n"
    "5:\n"
    "s_cmp_lt_u32 s24, 4\n"
    "s_cbranch_scc1 4b\n"
    "s_cmp_gt_u32 s39, 1\n"
    "s_cbranch_scc1 6f\n"
    "s_mov_b32 m0, s34\n"
    "s_nop 0\n"
    "global_load_lds_dwordx4 v8, s[26:27] offset:0 sc1\n"
    "global_load_lds_dwordx4 v8, s[26:27] offset:1024 sc1\n"
    "global_load_lds_dwordx4 v8, s[26:27] offset:2048 sc1\n"
    "global_load_lds_dwordx4 v8, s[26:27] offset:3072 sc1\n"
    "s_add_u32 m0, s34, 1024\n"
    "s_add_u32 s26, s26, 1024\n"
    "s_addc_u32 s27, s27, 0\n"
    "global_load_lds_dwordx4 v8, s[26:27] offset:3072 sc1\n"
    "s_add_u32 s34, s34, 20480\n"
    "s_add_u32 s26, s26, 19456\n"
    "s_addc_u32 s27, s27, 0\n"
    "6:\n"
    "s_waitcnt vmcnt(0)\n"
    "s_barrier\n"
    "s_mov_b32 s24, 0x40000\n"
    "7:\n"
    "global_load_dwordx2 v[14:15], v12, s[40:41] sc1\n"
    "s_waitcnt vmcnt(0)\n"
    "v_cmp_ne_u64 vcc, s[42:43], v[14:15]\n"
    "s_cbranch_vccz 8f\n"
    "s_sleep 8\n"
    "s_sub_u32 s24, s24, 1\n"
    "s_cbranch_scc0 7b\n"
    "8:\n"
    "global_load_dwordx4 v[96:99], v8, s[28:29] offset:-2048 sc1\n"
    "global_load_dwordx4 v[100:103], v8, s[28:29] offset:-1024 sc1\n"
    "global_load_dwordx4 v[104:107], v8, s[28:29] offset:0 sc1\n"
    "global_load_dwordx4 v[108:111], v8, s[28:29] offset:1024 sc1\n"
    "global_load_dwordx4 v[112:115], v8, s[28:29] offset:2048 sc1\n"
    "global_load_dwordx4 v[116:119], v8, s[18:19] sc1\n"
    "global_load_dwordx4 v[120:123], v9, s[18:19] sc1\n"
    "global_load_dwordx4 v[124:127], v10, s[12:13] sc1\n"
    "global_load_dwordx4 v[128:131], v11, s[12:13] sc1\n"
    "s_add_u32 s28, s28, 20480\n"
    "s_addc_u32 s29, s29, 0\n"
    "s_add_u32 s18, s18, 4096\n"
    "s_addc_u32 s19, s19, 0\n"
    "s_add_u32 s12, s12, 4096\n"
    "s_addc_u32 s13, s13, 0\n"
    "s_mul_i32 s34, s39, 25600\n"
    "s_add_u32 s26, s30, s34\n"
    "s_addc_u32 s27, s31, 0\n"
    "s_mov_b32 s24, 0\n"
    "v_add_u32 v4, s10, v13\n"
    "v_add_u32 v6, s11, v13\n"
    "s_waitcnt vmcnt(0)\n"
    "global_load_dwordx4 v[132:135], v8, s[28:29] offset:-2048 sc1\n"
    "global_load_dwordx4 v[136:139], v8, s[28:29] offset:-1024 sc1\n"
    "v_mfma_f32_16x16x32_bf16 v[16:19], v[96:99], v[116:119], 0\n"
    "s_setprio 3\n"
    "v_mfma_f32_16x16x32_bf16 v[20:23], v[100:103], v[116:119], 0\n"
    "v_mfma_f32_16x16x32_bf16 v[24:27], v[104:107], v[116:119], 0\n"
    "s_setprio 2\n"
    "global_load_dwordx4 v[140:143], v8, s[28:29] offset:0 sc1\n"
    "global_load_dwordx4 v[144:147], v8, s[28:29] offset:1024 sc1\n"
    "v_mfma_f32_16x16x32_bf16 v[28:31], v[108:111], v[116:119], 0\n"
    "s_setprio 3\n"
    "v_mfma_f32_16x16x32_bf16 v[32:35], v[112:115], v[116:119], 0\n"
    "s_setprio 2\n"
    "global_load_dwordx4 v[148:151], v8, s[28:29] offset:2048 sc1\n"
    "global_load_dwordx4 v[152:155], v8, s[18:19] sc1\n"
    "v_mfma_f32_16x16x32_bf16 v[52:55], v[112:115], v[120:123], 0\n"
    "s_setprio 3\n"
    "v_mfma_f32_16x16x32_bf16 v[48:51], v[108:111], v[120:123], 0\n"
    "v_mfma_f32_16x16x32_bf16 v[44:47], v[104:107], v[120:123], 0\n"
    "s_setprio 2\n"
    "global_load_dwordx4 v[156:159], v9, s[18:19] sc1\n"
    "global_load_dwordx4 v[160:163], v10, s[12:13] sc1\n"
    "v_mfma_f32_16x16x32_bf16 v[40:43], v[100:103], v[120:123], 0\n"
    "s_setprio 3\n"
    "v_mfma_f32_16x16x32_bf16 v[36:39], v[96:99], v[120:123], 0\n"
    "s_setprio 2\n"
    "global_load_dwordx4 v[164:167], v11, s[12:13] sc1\n"
    "s_add_u32 s28, s28, 20480\n"
    "s_addc_u32 s29, s29, 0\n"
    "s_add_u32 s18, s18, 4096\n"
    "s_addc_u32 s19, s19, 0\n"
    "s_add_u32 s12, s12, 4096\n"
    "s_addc_u32 s13, s13, 0\n"
    "v_mfma_f32_16x16x32_bf16 v[56:59], v[96:99], v[124:127], 0\n"
    "s_setprio 3\n"
    "v_mfma_f32_16x16x32_bf16 v[60:63], v[100:103], v[124:127], 0\n"
    "v_mfma_f32_16x16x32_bf16 v[64:67], v[104:107], v[124:127], 0\n"
    "s_setprio 2\n"
    "v_mfma_f32_16x16x32_bf16 v[68:71], v[108:111], v[124:127], 0\n"
    "s_setprio 3\n"
    "v_mfma_f32_16x16x32_bf16 v[72:75], v[112:115], v[124:127], 0\n"
    "s_setprio 2\n"
    "v_mfma_f32_16x16x32_bf16 v[92:95], v[112:115], v[128:131], 0\n"
    "s_setprio 3\n"
    "v_mfma_f32_16x16x32_bf16 v[88:91], v[108:111], v[128:131], 0\n"
    "v_mfma_f32_16x16x32_bf16 v[84:87], v[104:107], v[128:131], 0\n"
    "s_setprio 2\n"
    "v_mfma_f32_16x16x32_bf16 v[80:83], v[100:103], v[128:131], 0\n"
    "s_setprio 3\n"
    "v_mfma_f32_16x16x32_bf16 v[76:79], v[96:99], v[128:131], 0\n"
    "s_setprio 2\n"
    "9:\n"
    "s_waitcnt vmcnt(0)\n"
    "global_load_dwordx4 v[96:99], v8, s[28:29] offset:-2048 sc1\n"
    "global_load_dwordx4 v[100:103], v8, s[28:29] offset:-1024 sc1\n"
    "v_mfma_f32_16x16x32_bf16 v[16:19], v[132:135], v[152:155], v[16:19]\n"
    "s_setprio 3\n"
    "v_mfma_f32_16x16x32_bf16 v[20:23], v[136:139], v[152:155], v[20:23]\n"
    "v_mfma_f32_16x16x32_bf16 v[24:27], v[140:143], v[152:155], v[24:27]\n"
    "s_setprio 2\n"
    "global_load_dwordx4 v[104:107], v8, s[28:29] offset:0 sc1\n"
    "global_load_dwordx4 v[108:111], v8, s[28:29] offset:1024 sc1\n"
    "v_mfma_f32_16x16x32_bf16 v[28:31], v[144:147], v[152:155], v[28:31]\n"
    "s_setprio 3\n"
    "v_mfma_f32_16x16x32_bf16 v[32:35], v[148:151], v[152:155], v[32:35]\n"
    "s_setprio 2\n"
    "global_load_dwordx4 v[112:115], v8, s[28:29] offset:2048 sc1\n"
    "global_load_dwordx4 v[116:119], v8, s[18:19] sc1\n"
    "v_mfma_f32_16x16x32_bf16 v[52:55], v[148:151], v[156:159], v[52:55]\n"
    "s_setprio 3\n"
    "v_mfma_f32_16x16x32_bf16 v[48:51], v[144:147], v[156:159], v[48:51]\n"
    "v_mfma_f32_16x16x32_bf16 v[44:47], v[140:143], v[156:159], v[44:47]\n"
    "s_setprio 2\n"
    "global_load_dwordx4 v[120:123], v9, s[18:19] sc1\n"
    "global_load_dwordx4 v[124:127], v10, s[12:13] sc1\n"
    "v_mfma_f32_16x16x32_bf16 v[40:43], v[136:139], v[156:159], v[40:43]\n"
    "s_setprio 3\n"
    "v_mfma_f32_16x16x32_bf16 v[36:39], v[132:135], v[156:159], v[36:39]\n"
    "s_setprio 2\n"
    "global_load_dwordx4 v[128:131], v11, s[12:13] sc1\n"
    "s_add_u32 s28, s28, 20480\n"
    "s_addc_u32 s29, s29, 0\n"
    "s_add_u32 s18, s18, 4096\n"
    "s_addc_u32 s19, s19, 0\n"
    "s_add_u32 s12, s12, 4096\n"
    "s_addc_u32 s13, s13, 0\n"
    "v_mfma_f32_16x16x32_bf16 v[56:59], v[132:135], v[160:163], v[56:59]\n"
    "s_setprio 3\n"
    "v_mfma_f32_16x16x32_bf16 v[60:63], v[136:139], v[160:163], v[60:63]\n"
    "v_mfma_f32_16x16x32_bf16 v[64:67], v[140:143], v[160:163], v[64:67]\n"
    "s_setprio 2\n"
    "v_mfma_f32_16x16x32_bf16 v[68:71], v[144:147], v[160:163], v[68:71]\n"
    "s_setprio 3\n"
    "v_mfma_f32_16x16x32_bf16 v[72:75], v[148:151], v[160:163], v[72:75]\n"
    "s_setprio 2\n"
    "v_mfma_f32_16x16x32_bf16 v[92:95], v[148:151], v[164:167], v[92:95]\n"
    "s_setprio 3\n"
    "v_mfma_f32_16x16x32_bf16 v[88:91], v[144:147], v[164:167], v[88:91]\n"
    "v_mfma_f32_16x16x32_bf16 v[84:87], v[140:143], v[164:167], v[84:87]\n"
    "s_setprio 2\n"
    "v_mfma_f32_16x16x32_bf16 v[80:83], v[136:139], v[164:167], v[80:83]\n"
    "s_setprio 3\n"
    "v_mfma_f32_16x16x32_bf16 v[76:79], v[132:135], v[164:167], v[76:79]\n"
    "s_setprio 2\n"
    "s_add_u32 s24, s24, 1\n"
    "s_cmp_eq_u32 s24, 4\n"
    "s_cbranch_scc1 15f\n"
    "s_waitcnt vmcnt(0)\n"
    "global_load_dwordx4 v[132:135], v8, s[28:29] offset:-2048 sc1\n"
    "global_load_dwordx4 v[136:139], v8, s[28:29] offset:-1024 sc1\n"
    "v_mfma_f32_16x16x32_bf16 v[16:19], v[96:99], v[116:119], v[16:19]\n"
    "s_setprio 3\n"
    "v_mfma_f32_16x16x32_bf16 v[20:23], v[100:103], v[116:119], v[20:23]\n"
    "v_mfma_f32_16x16x32_bf16 v[24:27], v[104:107], v[116:119], v[24:27]\n"
    "s_setprio 2\n"
    "global_load_dwordx4 v[140:143], v8, s[28:29] offset:0 sc1\n"
    "global_load_dwordx4 v[144:147], v8, s[28:29] offset:1024 sc1\n"
    "v_mfma_f32_16x16x32_bf16 v[28:31], v[108:111], v[116:119], v[28:31]\n"
    "s_setprio 3\n"
    "v_mfma_f32_16x16x32_bf16 v[32:35], v[112:115], v[116:119], v[32:35]\n"
    "s_setprio 2\n"
    "global_load_dwordx4 v[148:151], v8, s[28:29] offset:2048 sc1\n"
    "global_load_dwordx4 v[152:155], v8, s[18:19] sc1\n"
    "v_mfma_f32_16x16x32_bf16 v[52:55], v[112:115], v[120:123], v[52:55]\n"
    "s_setprio 3\n"
    "v_mfma_f32_16x16x32_bf16 v[48:51], v[108:111], v[120:123], v[48:51]\n"
    "v_mfma_f32_16x16x32_bf16 v[44:47], v[104:107], v[120:123], v[44:47]\n"
    "s_setprio 2\n"
    "global_load_dwordx4 v[156:159], v9, s[18:19] sc1\n"
    "global_load_dwordx4 v[160:163], v10, s[12:13] sc1\n"
    "v_mfma_f32_16x16x32_bf16 v[40:43], v[100:103], v[120:123], v[40:43]\n"
    "s_setprio 3\n"
    "v_mfma_f32_16x16x32_bf16 v[36:39], v[96:99], v[120:123], v[36:39]\n"
    "s_setprio 2\n"
    "global_load_dwordx4 v[164:167], v11, s[12:13] sc1\n"
    "s_add_u32 s28, s28, 20480\n"
    "s_addc_u32 s29, s29, 0\n"
    "s_add_u32 s18, s18, 4096\n"
    "s_addc_u32 s19, s19, 0\n"
    "s_add_u32 s12, s12, 4096\n"
    "s_addc_u32 s13, s13, 0\n"
    "v_mfma_f32_16x16x32_bf16 v[56:59], v[96:99], v[124:127], v[56:59]\n"
    "s_setprio 3\n"
    "v_mfma_f32_16x16x32_bf16 v[60:63], v[100:103], v[124:127], v[60:63]\n"
    "v_mfma_f32_16x16x32_bf16 v[64:67], v[104:107], v[124:127], v[64:67]\n"
    "s_setprio 2\n"
    "v_mfma_f32_16x16x32_bf16 v[68:71], v[108:111], v[124:127], v[68:71]\n"
    "s_setprio 3\n"
    "v_mfma_f32_16x16x32_bf16 v[72:75], v[112:115], v[124:127], v[72:75]\n"
    "s_setprio 2\n"
    "v_mfma_f32_16x16x32_bf16 v[92:95], v[112:115], v[128:131], v[92:95]\n"
    "s_setprio 3\n"
    "v_mfma_f32_16x16x32_bf16 v[88:91], v[108:111], v[128:131], v[88:91]\n"
    "v_mfma_f32_16x16x32_bf16 v[84:87], v[104:107], v[128:131], v[84:87]\n"
    "s_setprio 2\n"
    "v_mfma_f32_16x16x32_bf16 v[80:83], v[100:103], v[128:131], v[80:83]\n"
    "s_setprio 3\n"
    "v_mfma_f32_16x16x32_bf16 v[76:79], v[96:99], v[128:131], v[76:79]\n"
    "s_setprio 2\n"
    "s_branch 9b\n"
    "15:\n"
    "s_barrier\n"
    "s_waitcnt vmcnt(0)\n"
    "global_load_dwordx4 v[132:135], v8, s[28:29] offset:-2048 sc1\n"
    "global_load_dwordx4 v[136:139], v8, s[28:29] offset:-1024 sc1\n"
    "global_load_dwordx4 v[140:143], v8, s[28:29] offset:0 sc1\n"
    "v_mfma_f32_16x16x32_bf16 v[16:19], v[96:99], v[116:119], v[16:19]\n"
    "s_setprio 3\n"
    "v_mfma_f32_16x16x32_bf16 v[20:23], v[100:103], v[116:119], v[20:23]\n"
    "v_mfma_f32_16x16x32_bf16 v[24:27], v[104:107], v[116:119], v[24:27]\n"
    "s_setprio 2\n"
    "global_load_dwordx4 v[144:147], v8, s[28:29] offset:1024 sc1\n"
    "global_load_dwordx4 v[148:151], v8, s[28:29] offset:2048 sc1\n"
    "global_load_dwordx4 v[152:155], v8, s[18:19] sc1\n"
    "v_mfma_f32_16x16x32_bf16 v[28:31], v[108:111], v[116:119], v[28:31]\n"
    "s_setprio 3\n"
    "v_mfma_f32_16x16x32_bf16 v[32:35], v[112:115], v[116:119], v[32:35]\n"
    "s_setprio 2\n"
    "global_load_dwordx4 v[156:159], v9, s[18:19] sc1\n"
    "global_load_dwordx4 v[160:163], v10, s[12:13] sc1\n"
    "global_load_dwordx4 v[164:167], v11, s[12:13] sc1\n"
    "s_add_u32 s28, s28, 20480\n"
    "s_addc_u32 s29, s29, 0\n"
    "s_add_u32 s18, s18, 4096\n"
    "s_addc_u32 s19, s19, 0\n"
    "s_add_u32 s12, s12, 4096\n"
    "s_addc_u32 s13, s13, 0\n"
    "v_mfma_f32_16x16x32_bf16 v[52:55], v[112:115], v[120:123], v[52:55]\n"
    "s_setprio 3\n"
    "v_mfma_f32_16x16x32_bf16 v[48:51], v[108:111], v[120:123], v[48:51]\n"
    "v_mfma_f32_16x16x32_bf16 v[44:47], v[104:107], v[120:123], v[44:47]\n"
    "s_setprio 2\n"
    "s_mov_b32 m0, s34\n"
    "s_nop 0\n"
    "global_load_lds_dwordx4 v8, s[26:27] offset:0 sc1\n"
    "global_load_lds_dwordx4 v8, s[26:27] offset:1024 sc1\n"
    "global_load_lds_dwordx4 v8, s[26:27] offset:2048 sc1\n"
    "v_mfma_f32_16x16x32_bf16 v[40:43], v[100:103], v[120:123], v[40:43]\n"
    "s_setprio 3\n"
    "v_mfma_f32_16x16x32_bf16 v[36:39], v[96:99], v[120:123], v[36:39]\n"
    "s_setprio 2\n"
    "global_load_lds_dwordx4 v8, s[26:27] offset:3072 sc1\n"
    "s_add_u32 s34, s34, 4096\n"
    "s_add_u32 s26, s26, 4096\n"
    "s_addc_u32 s27, s27, 0\n"
    "s_mov_b32 m0, s34\n"
    "s_nop 0\n"
    "global_load_lds_dwordx4 v8, s[26:27] offset:0 sc1\n"
    "global_load_lds_dwordx4 v8, s[26:27] offset:1024 sc1\n"
    "v_mfma_f32_16x16x32_bf16 v[56:59], v[96:99], v[124:127], v[56:59]\n"
    "s_setprio 3\n"
    "v_mfma_f32_16x16x32_bf16 v[60:63], v[100:103], v[124:127], v[60:63]\n"
    "v_mfma_f32_16x16x32_bf16 v[64:67], v[104:107], v[124:127], v[64:67]\n"
    "s_setprio 2\n"
    "global_load_lds_dwordx4 v8, s[26:27] offset:2048 sc1\n"
    "global_load_lds_dwordx4 v8, s[26:27] offset:3072 sc1\n"
    "s_add_u32 s34, s34, 4096\n"
    "s_add_u32 s26, s26, 4096\n"
    "s_addc_u32 s27, s27, 0\n"
    "s_mov_b32 m0, s34\n"
    "s_nop 0\n"
    "global_load_lds_dwordx4 v8, s[26:27] offset:0 sc1\n"
    "v_mfma_f32_16x16x32_bf16 v[68:71], v[108:111], v[124:127], v[68:71]\n"
    "s_setprio 3\n"
    "v_mfma_f32_16x16x32_bf16 v[72:75], v[112:115], v[124:127], v[72:75]\n"
    "s_setprio 2\n"
    "global_load_lds_dwordx4 v8, s[26:27] offset:1024 sc1\n"
    "global_load_lds_dwordx4 v8, s[26:27] offset:2048 sc1\n"
    "global_load_lds_dwordx4 v8, s[26:27] offset:3072 sc1\n"
    "s_add_u32 s34, s34, 4096\n"
    "s_add_u32 s26, s26, 4096\n"
    "s_addc_u32 s27, s27, 0\n"
    "v_mfma_f32_16x16x32_bf16 v[92:95], v[112:115], v[128:131], v[92:95]\n"
    "s_setprio 3\n"
    "v_mfma_f32_16x16x32_bf16 v[88:91], v[108:111], v[128:131], v[88:91]\n"
    "v_mfma_f32_16x16x32_bf16 v[84:87], v[104:107], v[128:131], v[84:87]\n"
    "s_setprio 2\n"
    "v_mfma_f32_16x16x32_bf16 v[80:83], v[100:103], v[128:131], v[80:83]\n"
    "s_setprio 3\n"
    "v_mfma_f32_16x16x32_bf16 v[76:79], v[96:99], v[128:131], v[76:79]\n"
    "s_setprio 2\n"
    "s_waitcnt vmcnt(12)\n"
    "global_load_dwordx4 v[96:99], v8, s[28:29] offset:-2048 sc1\n"
    "global_load_dwordx4 v[100:103], v8, s[28:29] offset:-1024 sc1\n"
    "global_load_dwordx4 v[104:107], v8, s[28:29] offset:0 sc1\n"
    "global_load_dwordx4 v[108:111], v8, s[28:29] offset:1024 sc1\n"
    "v_mfma_f32_16x16x32_bf16 v[16:19], v[132:135], v[152:155], v[16:19]\n"
    "s_setprio 3\n"
    "v_mfma_f32_16x16x32_bf16 v[20:23], v[136:139], v[152:155], v[20:23]\n"
    "v_mfma_f32_16x16x32_bf16 v[24:27], v[140:143], v[152:155], v[24:27]\n"
    "s_setprio 2\n"
    "global_load_dwordx4 v[112:115], v8, s[28:29] offset:2048 sc1\n"
    "global_load_dwordx4 v[116:119], v8, s[18:19] sc1\n"
    "global_load_dwordx4 v[120:123], v9, s[18:19] sc1\n"
    "global_load_dwordx4 v[124:127], v10, s[12:13] sc1\n"
    "v_mfma_f32_16x16x32_bf16 v[28:31], v[144:147], v[152:155], v[28:31]\n"
    "s_setprio 3\n"
    "v_mfma_f32_16x16x32_bf16 v[32:35], v[148:151], v[152:155], v[32:35]\n"
    "s_setprio 2\n"
    "global_load_dwordx4 v[128:131], v11, s[12:13] sc1\n"
    "s_add_u32 s28, s28, 20480\n"
    "s_addc_u32 s29, s29, 0\n"
    "s_add_u32 s18, s18, 4096\n"
    "s_addc_u32 s19, s19, 0\n"
    "s_add_u32 s12, s12, 4096\n"
    "s_addc_u32 s13, s13, 0\n"
    "s_mov_b32 m0, s34\n"
    "s_nop 0\n"
    "global_load_lds_dwordx4 v8, s[26:27] offset:0 sc1\n"
    "global_load_lds_dwordx4 v8, s[26:27] offset:1024 sc1\n"
    "global_load_lds_dwordx4 v8, s[26:27] offset:2048 sc1\n"
    "v_mfma_f32_16x16x32_bf16 v[52:55], v[148:151], v[156:159], v[52:55]\n"
    "s_setprio 3\n"
    "v_mfma_f32_16x16x32_bf16 v[48:51], v[144:147], v[156:159], v[48:51]\n"
    "v_mfma_f32_16x16x32_bf16 v[44:47], v[140:143], v[156:159], v[44:47]\n"
    "s_setprio 2\n"
    "global_load_lds_dwordx4 v8, s[26:27] offset:3072 sc1\n"
    "s_add_u32 s34, s34, 4096\n"
    "s_add_u32 s26, s26, 4096\n"
    "s_addc_u32 s27, s27, 0\n"
    "s_mov_b32 m0, s34\n"
    "s_nop 0\n"
    "global_load_lds_dwordx4 v8, s[26:27] offset:0 sc1\n"
    "global_load_lds_dwordx4 v8, s[26:27] offset:1024 sc1\n"
    "global_load_lds_dwordx4 v8, s[26:27] offset:2048 sc1\n"
    "v_mfma_f32_16x16x32_bf16 v[40:43], v[136:139], v[156:159], v[40:43]\n"
    "s_setprio 3\n"
    "v_mfma_f32_16x16x32_bf16 v[36:39], v[132:135], v[156:159], v[36:39]\n"
    "s_setprio 2\n"
    "global_load_lds_dwordx4 v8, s[26:27] offset:3072 sc1\n"
    "s_add_u32 s34, s34, 4096\n"
    "s_add_u32 s26, s26, 4096\n"
    "s_addc_u32 s27, s27, 0\n"
    "s_mov_b32 m0, s34\n"
    "s_nop 0\n"
    "global_load_lds_dwordx4 v8, s[26:27] offset:0 sc1\n"
    "global_load_lds_dwordx4 v8, s[26:27] offset:1024 sc1\n"
    "global_load_lds_dwordx4 v8, s[26:27] offset:2048 sc1\n"
    "v_mfma_f32_16x16x32_bf16 v[56:59], v[132:135], v[160:163], v[56:59]\n"
    "s_setprio 3\n"
    "v_mfma_f32_16x16x32_bf16 v[60:63], v[136:139], v[160:163], v[60:63]\n"
    "v_mfma_f32_16x16x32_bf16 v[64:67], v[140:143], v[160:163], v[64:67]\n"
    "s_setprio 2\n"
    "global_load_lds_dwordx4 v8, s[26:27] offset:3072 sc1\n"
    "s_add_u32 s34, s34, 4096\n"
    "s_add_u32 s26, s26, 4096\n"
    "s_addc_u32 s27, s27, 0\n"
    "s_mov_b32 m0, s34\n"
    "s_nop 0\n"
    "global_load_lds_dwordx4 v8, s[26:27] offset:0 sc1\n"
    "v_mfma_f32_16x16x32_bf16 v[68:71], v[144:147], v[160:163], v[68:71]\n"
    "s_setprio 3\n"
    "v_mfma_f32_16x16x32_bf16 v[72:75], v[148:151], v[160:163], v[72:75]\n"
    "s_setprio 2\n"
    "v_mfma_f32_16x16x32_bf16 v[92:95], v[148:151], v[164:167], v[92:95]\n"
    "s_setprio 3\n"
    "v_mfma_f32_16x16x32_bf16 v[88:91], v[144:147], v[164:167], v[88:91]\n"
    "v_mfma_f32_16x16x32_bf16 v[84:87], v[140:143], v[164:167], v[84:87]\n"
    "s_setprio 2\n"
    "v_mfma_f32_16x16x32_bf16 v[80:83], v[136:139], v[164:167], v[80:83]\n"
    "s_setprio 3\n"
    "v_mfma_f32_16x16x32_bf16 v[76:79], v[132:135], v[164:167], v[76:79]\n"
    "s_setprio 2\n"
    "global_load_dwordx4 v[132:135], v8, s[28:29] offset:-2048 sc1\n"
    "global_load_dwordx4 v[136:139], v8, s[28:29] offset:-1024 sc1\n"
    "global_load_dwordx4 v[140:143], v8, s[28:29] offset:0 sc1\n"
    "global_load_dwordx4 v[144:147], v8, s[28:29] offset:1024 sc1\n"
    "global_load_dwordx4 v[148:151], v8, s[28:29] offset:2048 sc1\n"
    "global_load_dwordx4 v[152:155], v8, s[18:19] sc1\n"
    "global_load_dwordx4 v[156:159], v9, s[18:19] sc1\n"
    "global_load_dwordx4 v[160:163], v10, s[12:13] sc1\n"
    "global_load_dwordx4 v[164:167], v11, s[12:13] sc1\n"
    "s_add_u32 s28, s28, 20480\n"
    "s_addc_u32 s29, s29, 0\n"
    "s_add_u32 s18, s18, 4096\n"
    "s_addc_u32 s19, s19, 0\n"
    "s_add_u32 s12, s12, 4096\n"
    "s_addc_u32 s13, s13, 0\n"
    "v_lshrrev_b32 v14, 5, v8\n"
    "v_lshlrev_b32 v14, 7, v14\n"
    "v_bfe_u32 v15, v8, 4, 1\n"
    "v_lshl_add_u32 v14, v15, 2, v14\n"
    "s_mov_b32 s24, 0x40000\n"
    "v_mov_b32 v15, 0\n"
    "s_waitcnt vmcnt(0)\n"
    "s_barrier\n"
    "s_waitcnt vmcnt(0)\n"
    "v_mfma_f32_16x16x32_bf16 v[16:19], v[96:99], v[116:119], v[16:19]\n"
    "v_mfma_f32_16x16x32_bf16 v[20:23], v[100:103], v[116:119], v[20:23]\n"
    "v_mfma_f32_16x16x32_bf16 v[24:27], v[104:107], v[116:119], v[24:27]\n"
    "v_mfma_f32_16x16x32_bf16 v[28:31], v[108:111], v[116:119], v[28:31]\n"
    "v_mfma_f32_16x16x32_bf16 v[32:35], v[112:115], v[116:119], v[32:35]\n"
    "v_mfma_f32_16x16x32_bf16 v[52:55], v[112:115], v[120:123], v[52:55]\n"
    "v_mfma_f32_16x16x32_bf16 v[48:51], v[108:111], v[120:123], v[48:51]\n"
    "v_mfma_f32_16x16x32_bf16 v[44:47], v[104:107], v[120:123], v[44:47]\n"
    "v_mfma_f32_16x16x32_bf16 v[40:43], v[100:103], v[120:123], v[40:43]\n"
    "v_mfma_f32_16x16x32_bf16 v[36:39], v[96:99], v[120:123], v[36:39]\n"
    "v_mfma_f32_16x16x32_bf16 v[56:59], v[96:99], v[124:127], v[56:59]\n"
    "v_mfma_f32_16x16x32_bf16 v[60:63], v[100:103], v[124:127], v[60:63]\n"
    "v_mfma_f32_16x16x32_bf16 v[64:67], v[104:107], v[124:127], v[64:67]\n"
    "v_mfma_f32_16x16x32_bf16 v[68:71], v[108:111], v[124:127], v[68:71]\n"
    "v_mfma_f32_16x16x32_bf16 v[72:75], v[112:115], v[124:127], v[72:75]\n"
    "v_mfma_f32_16x16x32_bf16 v[92:95], v[112:115], v[128:131], v[92:95]\n"
    "v_mfma_f32_16x16x32_bf16 v[88:91], v[108:111], v[128:131], v[88:91]\n"
    "v_mfma_f32_16x16x32_bf16 v[84:87], v[104:107], v[128:131], v[84:87]\n"
    "v_mfma_f32_16x16x32_bf16 v[80:83], v[100:103], v[128:131], v[80:83]\n"
    "v_mfma_f32_16x16x32_bf16 v[76:79], v[96:99], v[128:131], v[76:79]\n"
    "s_cmp_gt_u32 s39, 1\n"
    "s_cbranch_scc1 21f\n"
    "s_lshl_b32 s50, s39, 6\n"
    "s_add_u32 s50, s50, 512\n"
    "v_lshrrev_b32 v0, 4, v8\n"
    "v_add_u32 v0, s50, v0\n"
    "v_mul_u32_u24 v1, 0xcccd, v0\n"
    "v_lshrrev_b32 v1, 19, v1\n"
    "v_mul_u32_u24 v2, 10, v1\n"
    "v_sub_u32 v0, v0, v2\n"
    "v_lshlrev_b32 v2, 4, v0\n"
    "v_mul_u32_u24 v12, 1280, v1\n"
    "v_add3_u32 v12, v12, v2, s20\n"
    "v_lshlrev_b32 v3, 4, v1\n"
    "global_load_dwordx4 v[96:99], v12, s[16:17]\n"
    "global_load_dwordx4 v[100:103], v2, s[14:15]\n"
    "global_load_dword v5, v3, s[22:23] sc1\n"
    "v_mul_u32_u24 v13, 320, v1\n"
    "v_lshlrev_b32 v1, 3, v1\n"
    "v_and_b32 v1, 48, v1\n"
    "v_lshlrev_b32 v0, 5, v0\n"
    "v_xor_b32 v0, v0, v1\n"
    "v_add_u32 v13, v13, v0\n"
    "s_cmp_lg_u32 s39, 0\n"
    "s_cbranch_scc1 21f\n"
    "global_load_dword v15, v14, s[46:47] sc1\n"
    "21:\n"
    "s_waitcnt vmcnt(63)\n"
    "v_mfma_f32_16x16x32_bf16 v[16:19], v[132:135], v[152:155], v[16:19]\n"
    "v_mfma_f32_16x16x32_bf16 v[20:23], v[136:139], v[152:155], v[20:23]\n"
    "v_mfma_f32_16x16x32_bf16 v[24:27], v[140:143], v[152:155], v[24:27]\n"
    "v_mfma_f32_16x16x32_bf16 v[28:31], v[144:147], v[152:155], v[28:31]\n"
    "v_mfma_f32_16x16x32_bf16 v[32:35], v[148:151], v[152:155], v[32:35]\n"
    "v_mfma_f32_16x16x32_bf16 v[52:55], v[148:151], v[156:159], v[52:55]\n"
    "v_mfma_f32_16x16x32_bf16 v[48:51], v[144:147], v[156:159], v[48:51]\n"
    "v_mfma_f32_16x16x32_bf16 v[44:47], v[140:143], v[156:159], v[44:47]\n"
    "v_mfma_f32_16x16x32_bf16 v[40:43], v[136:139], v[156:159], v[40:43]\n"
    "v_mfma_f32_16x16x32_bf16 v[36:39], v[132:135], v[156:159], v[36:39]\n"
    "v_mfma_f32_16x16x32_bf16 v[56:59], v[132:135], v[160:163], v[56:59]\n"
    "v_mfma_f32_16x16x32_bf16 v[60:63], v[136:139], v[160:163], v[60:63]\n"
    "v_mfma_f32_16x16x32_bf16 v[64:67], v[140:143], v[160:163], v[64:67]\n"
    "v_mfma_f32_16x16x32_bf16 v[68:71], v[144:147], v[160:163], v[68:71]\n"
    "v_mfma_f32_16x16x32_bf16 v[72:75], v[148:151], v[160:163], v[72:75]\n"
    "v_mfma_f32_16x16x32_bf16 v[92:95], v[148:151], v[164:167], v[92:95]\n"
    "v_mfma_f32_16x16x32_bf16 v[88:91], v[144:147], v[164:167], v[88:91]\n"
    "v_mfma_f32_16x16x32_bf16 v[84:87], v[140:143], v[164:167], v[84:87]\n"
    "v_mfma_f32_16x16x32_bf16 v[80:83], v[136:139], v[164:167], v[80:83]\n"
    "v_mfma_f32_16x16x32_bf16 v[76:79], v[132:135], v[164:167], v[76:79]\n"
    "ds_write_b128 v4, v[16:19] offset:0\n"
    "ds_write_b128 v4, v[20:23] offset:64\n"
    "ds_write_b128 v4, v[24:27] offset:128\n"
    "ds_write_b128 v4, v[28:31] offset:192\n"
    "ds_write_b128 v4, v[32:35] offset:256\n"
    "ds_write_b128 v4, v[36:39] offset:5120\n"
    "ds_write_b128 v4, v[40:43] offset:5184\n"
    "ds_write_b128 v4, v[44:47] offset:5248\n"
    "ds_write_b128 v4, v[48:51] offset:5312\n"
    "ds_write_b128 v4, v[52:55] offset:5376\n"
    "17:\n"
    "s_waitcnt vmcnt(0)\n"
    "v_cmp_eq_u32 vcc, -1, v15\n"
    "s_cbranch_vccz 18f\n"
    "s_sleep 2\n"
    "s_mov_b64 exec, vcc\n"
    "global_load_dword v15, v14, s[46:47] sc1\n"
    "s_mov_b64 exec, -1\n"
    "s_sub_u32 s24, s24, 1\n"
    "s_cbranch_scc0 17b\n"
    "s_waitcnt vmcnt(0)\n"
    "18:\n"
    "s_waitcnt lgkmcnt(0)\n"
    "s_barrier\n"
    "s_setprio 0\n"
    "ds_read_b128 v[104:107], v6 offset:0\n"
    "ds_read_b128 v[108:111], v6 offset:64\n"
    "ds_read_b128 v[112:115], v6 offset:128\n"
    "ds_read_b128 v[116:119], v6 offset:192\n"
    "ds_read_b128 v[120:123], v6 offset:256\n"
    "ds_read_b128 v[124:127], v6 offset:5120\n"
    "ds_read_b128 v[128:131], v6 offset:5184\n"
    "ds_read_b128 v[132:135], v6 offset:5248\n"
    "ds_read_b128 v[136:139], v6 offset:5312\n"
    "ds_read_b128 v[140:143], v6 offset:5376\n"
    "v_lshlrev_b32 v16, 16, v96\n"
    "v_lshlrev_b32 v14, 16, v100\n"
    "v_add_f32 v16, v16, v14\n"
    "v_and_b32 v17, 0xffff0000, v96\n"
    "v_and_b32 v14, 0xffff0000, v100\n"
    "v_add_f32 v17, v17, v14\n"
    "v_lshlrev_b32 v18, 16, v97\n"
    "v_lshlrev_b32 v14, 16, v101\n"
    "v_add_f32 v18, v18, v14\n"
    "v_and_b32 v19, 0xffff0000, v97\n"
    "v_and_b32 v14, 0xffff0000, v101\n"
    "v_add_f32 v19, v19, v14\n"
    "v_lshlrev_b32 v20, 16, v98\n"
    "v_lshlrev_b32 v14, 16, v102\n"
    "v_add_f32 v20, v20, v14\n"
    "v_and_b32 v21, 0xffff0000, v98\n"
    "v_and_b32 v14, 0xffff0000, v102\n"
    "v_add_f32 v21, v21, v14\n"
    "v_lshlrev_b32 v22, 16, v99\n"
    "v_lshlrev_b32 v14, 16, v103\n"
    "v_add_f32 v22, v22, v14\n"
    "v_and_b32 v23, 0xffff0000, v99\n"
    "v_and_b32 v14, 0xffff0000, v103\n"
    "v_add_f32 v23, v23, v14\n"
    "v_mul_f32 v5, v5, v5\n"
    "s_waitcnt lgkmcnt(0)\n"
    "v_add_f32 v56, v56, v104\n"
    "v_add_f32 v57, v57, v105\n"
    "v_add_f32 v58, v58, v106\n"
    "v_add_f32 v59, v59, v107\n"
    "v_add_f32 v60, v60, v108\n"
    "v_add_f32 v61, v61, v109\n"
    "v_add_f32 v62, v62, v110\n"
    "v_add_f32 v63, v63, v111\n"
    "v_add_f32 v64, v64, v112\n"
    "v_add_f32 v65, v65, v113\n"
    "v_add_f32 v66, v66, v114\n"
    "v_add_f32 v67, v67, v115\n"
    "v_add_f32 v68, v68, v116\n"
    "v_add_f32 v69, v69, v117\n"
    "v_add_f32 v70, v70, v118\n"
    "v_add_f32 v71, v71, v119\n"
    "v_add_f32 v72, v72, v120\n"
    "v_add_f32 v73, v73, v121\n"
    "v_add_f32 v74, v74, v122\n"
    "v_add_f32 v75, v75, v123\n"
    "v_add_f32 v76, v76, v124\n"
    "v_add_f32 v77, v77, v125\n"
    "v_add_f32 v78, v78, v126\n"
    "v_add_f32 v79, v79, v127\n"
    "v_add_f32 v80, v80, v128\n"
    "v_add_f32 v81, v81, v129\n"
    "v_add_f32 v82, v82, v130\n"
    "v_add_f32 v83, v83, v131\n"
    "v_add_f32 v84, v84, v132\n"
    "v_add_f32 v85, v85, v133\n"
    "v_add_f32 v86, v86, v134\n"
    "v_add_f32 v87, v87, v135\n"
    "v_add_f32 v88, v88, v136\n"
    "v_add_f32 v89, v89, v137\n"
    "v_add_f32 v90, v90, v138\n"
    "v_add_f32 v91, v91, v139\n"
    "v_add_f32 v92, v92, v140\n"
    "v_add_f32 v93, v93, v141\n"
    "v_add_f32 v94, v94, v142\n"
    "v_add_f32 v95, v95, v143\n"
    "ds_write_b128 v6, v[56:59] offset:0\n"
    "ds_write_b128 v6, v[60:63] offset:64\n"
    "ds_write_b128 v6, v[64:67] offset:128\n"
    "ds_write_b128 v6, v[68:71] offset:192\n"
    "ds_write_b128 v6, v[72:75] offset:256\n"
    "ds_write_b128 v6, v[76:79] offset:5120\n"
    "ds_write_b128 v6, v[80:83] offset:5184\n"
    "ds_write_b128 v6, v[84:87] offset:5248\n"
    "ds_write_b128 v6, v[88:91] offset:5312\n"
    "ds_write_b128 v6, v[92:95] offset:5376\n"
        : "+{s[26:27]}"(kvp), "+{s[28:29]}"(wp), "+{s[18:19]}"(hp), "+{s[12:13]}"(hq), "+{v12}"(vf), "+{s34}"(mk), "+{v13}"(ra), "={v[16:23]}"(xb), "={v5}"(r2)
        : "{v[8:11]}"(vh), "{s[30:31]}"(wop), "{s10}"(wm * 20480 + (1 - wn) * 10240), "{s11}"(wm * 20480 + wn * 10240), "{s[14:15]}"(b2 + cf * 160), "{s[16:17]}"(x), "{s20}"(rt * 81920 + cf * 160), "{s[22:23]}"(ws + WS_ST + rt * 1024 + 8), "{s39}"(w4), "{s[40:41]}"(ws), "{s[42:43]}"(tag), "{s[44:45]}"(fh), "{s[46:47]}"(ws + WS_HINT + rt * 4096)
        : "v0","v1","v2","v3","v4","v6","v14","v15","v24","v25","v26","v27","v28","v29","v30","v31","v32","v33","v34","v35","v36","v37","v38","v39","v40","v41","v42","v43","v44","v45","v46","v47","v48","v49","v50","v51","v52","v53","v54","v55","v56","v57","v58","v59","v60","v61","v62","v63","v64","v65","v66","v67","v68","v69","v70","v71","v72","v73","v74","v75","v76","v77","v78","v79","v80","v81","v82","v83","v84","v85","v86","v87","v88","v89","v90","v91","v92","v93","v94","v95","v96","v97","v98","v99","v100","v101","v102","v103","v104","v105","v106","v107","v108","v109","v110","v111","v112","v113","v114","v115","v116","v117","v118","v119","v120","v121","v122","v123","v124","v125","v126","v127","v128","v129","v130","v131","v132","v133","v134","v135","v136","v137","v138","v139","v140","v141","v142","v143","v144","v145","v146","v147","v148","v149","v150","v151","v152","v153","v154","v155","v156","v157","v158","v159","v160","v161","v162","v163","v164","v165","v166","v167","s24","s35","s48","s49","s50","scc","memory","vcc");
      xo = vf; a0 = ra;
    }
    {
      if (b == 0 && w4 == 2) {
        LANE_VARS
        for (unsigned sp = 0; sp < SPIN_CAP; ++sp) { bool ok = true;
#pragma unroll
          for (int j = 0; j < 16; ++j) ok &= flag_load(ws + WS_FLAGA + j * 512 + lane * 8) == tag;
          if (__all(ok)) break; }
        if (lane == 0) flag_store(ws + WS_EPOCH, tag);
      }
    }
  }

  }
  bar();
  if (wid < 10) { const unsigned a1 = a0 ^ 16;
    const f32x4 s0 = *(const LAS f32x4*)a0 + *(const LAS f32x4*)(a0 + 51200) + ((*(const LAS f32x4*)(a0 + L_RED) + *(const LAS f32x4*)(a0 + L_RED + 20480)) * r2 + f32x4{xb[0], xb[1], xb[2], xb[3]});
    const f32x4 s1 = *(const LAS f32x4*)a1 + *(const LAS f32x4*)(a1 + 51200) + ((*(const LAS f32x4*)(a1 + L_RED) + *(const LAS f32x4*)(a1 + L_RED + 20480)) * r2 + f32x4{xb[4], xb[5], xb[6], xb[7]});
    const u32x4 v = {cvtpk(s0[0], s0[1]), cvtpk(s0[2], s0[3]), cvtpk(s1[0], s1[1]), cvtpk(s1[2], s1[3])};
    __builtin_amdgcn_raw_buffer_store_b128(v, __builtin_amdgcn_make_buffer_rsrc(out, 0, 2048 * 1280, 0x00020000), xo, 0, 16); }
}

extern "C" void kernel_launch(void* const* d_in, const int* in_sizes, int n_in, void* d_out, int out_size, void* d_ws, size_t ws_size, hipStream_t stream) {
  (void)in_sizes; (void)n_in; (void)out_size; (void)ws_size;
  (void)hipFuncSetAttribute((const void*)fused_block, hipFuncAttributeMaxDynamicSharedMemorySize, LDS_BYTES);
  fused_block<<<256, 768, LDS_BYTES, stream>>>((const char*)d_in[0], (const char*)d_in[1], (const char*)d_in[2], (const char*)d_in[4], (const char*)d_in[5],
                                                (const char*)d_in[6], (const char*)d_in[7], (char*)d_out, (char*)d_ws);
}
```
